```python
import torch

def _softmax(x, axis=-1):
    return torch.softmax(x, dim=axis)
def _gelu(x):
    return torch.nn.functional.gelu(x, approximate="tanh")
def _silu(x):
    return torch.nn.functional.silu(x)
def _sigmoid(x):
    return torch.sigmoid(x)
def _layernorm(x, axis=-1, eps=1e-5):
    m = x.mean(dim=axis, keepdim=True)
    v = x.var(dim=axis, keepdim=True, unbiased=False)
    return (x - m) / torch.sqrt(v + eps)
def _scatter_add(tgt, idx, src, axis=0):
    return tgt.clone().index_add_(axis, idx, src)
def _segment_sum(src, seg, n):
    out = torch.zeros((n,) + src.shape[1:], dtype=src.dtype)
    return out.index_add_(0, seg, src)
def _pad(x, pw):
    flat = tuple(v for pair in reversed(pw) for v in pair)
    return torch.nn.functional.pad(x, flat)

kernel_name = 'parallel_sum__linear_kernel_hedgehog__dense_activation_squared_relu__prompt'

def setup_inputs():

    _w = (lambda a: (lambda t: t.to(torch.bfloat16) if t.is_floating_point() else t)(torch.as_tensor(a)))
    def _A_setup():
        B, S, D, H = 1, 2048, 640, 32
        Dh = D // H
        return {
            "x": _w((lambda *s: torch.randn(*s, dtype=torch.bfloat16) * 0.05)(B, S, D)),
            "wqkv": _w((lambda *s: torch.randn(*s, dtype=torch.bfloat16) * 0.8)(D, 3 * D)),
            "w_phi": _w((lambda *s: torch.randn(*s, dtype=torch.bfloat16) * 0.8)(H, Dh, Dh) * (Dh ** -0.5)),
            "wo": _w((lambda *s: torch.randn(*s, dtype=torch.bfloat16) * 0.2)(D, D)),
        }
    _A_in = _A_setup()

    def _B_setup():
        B, S, D, F = 1, 2048, 640, 1728
        return {
            "x": _w((lambda *s: torch.randn(*s, dtype=torch.bfloat16) * 0.05)(B, S, D)),
            "w1": _w((lambda *s: torch.randn(*s, dtype=torch.bfloat16) * 0.05)(D, F)),
            "b1": _w((lambda *s: torch.randn(*s, dtype=torch.bfloat16) * 0.8)(F)),
            "w2": _w((lambda *s: torch.randn(*s, dtype=torch.bfloat16) * 0.05)(F, D)),
            "b2": _w((lambda *s: torch.randn(*s, dtype=torch.bfloat16) * 3.2)(D)),
        }
    _B_in = _B_setup()

    return {
        "A_x": _A_in["x"],
        "A_wqkv": _A_in["wqkv"],
        "A_w_phi": _A_in["w_phi"],
        "A_wo": _A_in["wo"],
        "B_x": _B_in["x"],
        "B_w1": _B_in["w1"],
        "B_b1": _B_in["b1"],
        "B_w2": _B_in["w2"],
        "B_b2": _B_in["b2"],
    }

def reference(A_x, A_wqkv, A_w_phi, A_wo, B_x, B_w1, B_b1, B_w2, B_b2):
    def _A_ref(x, wqkv, w_phi, wo):
        B, S, D = x.shape
        H = 32; Dh = D // H
        qkv = (lambda x, *d: x.permute(*d))((x @ wqkv).reshape(B, S, 3, H, Dh), 2, 0, 3, 1, 4)
        q, k, v = qkv[0], qkv[1], qkv[2]
        pq = torch.einsum("bhsd,hdf->bhsf", q, w_phi)
        pk = torch.einsum("bhsd,hdf->bhsf", k, w_phi)
        phi_q = torch.concatenate([_softmax(pq, axis=-1), _softmax(-pq, axis=-1)], axis=-1)
        phi_k = torch.concatenate([_softmax(pk, axis=-1), _softmax(-pk, axis=-1)], axis=-1)
        kv = torch.einsum("bhsf,bhsd->bhfd", phi_k, v)
        z  = torch.einsum("bhsf->bhf", phi_k)
        num = torch.einsum("bhqf,bhfd->bhqd", phi_q, kv)
        den = torch.einsum("bhqf,bhf->bhq", phi_q, z)[..., None] + 1e-6
        o = num / den
        return (lambda x, *d: x.permute(*d))(o, 0, 2, 1, 3).reshape(B, S, D) @ wo

    def _B_ref(x, w1, b1, w2, b2):
        h = torch.einsum("bsd,df->bsf", x, w1) + b1
        zero = torch.zeros_like(h)
        r = torch.where(h > zero, h, zero)
        h = r * r
        out = torch.einsum("bsf,fd->bsd", h, w2) + b2
        return out

    x = A_x
    xn = _layernorm(x, axis=-1)
    return x + _A_ref(xn, A_wqkv, A_w_phi, A_wo) + _B_ref(xn, B_w1, B_b1, B_w2, B_b2)
```

```cpp
#include <hip/hip_runtime.h>
#include <hip/hip_bf16.h>
typedef unsigned short u16;
__device__ __forceinline__ float b2f(u16 v){ return __uint_as_float(((unsigned)v)<<16); }
__device__ __forceinline__ u16 f2b(float f){ unsigned u=__float_as_uint(f); u += 0x7fffu + ((u>>16)&1u); return (u16)(u>>16); }
__device__ __forceinline__ float rb(float f){ return b2f(f2b(f)); }
constexpr int S=2048,D=640,H=32,DH=20,F=1728;

__global__ void k_ln(const u16* x, u16* xn){
  int r=blockIdx.x; int t=threadIdx.x;
  float s=0; for(int i=t;i<D;i+=64) s+=b2f(x[r*D+i]);
  for(int o=32;o;o>>=1) s+=__shfl_xor(s,o);
  float m=s/D; float q=0; for(int i=t;i<D;i+=64){float d=b2f(x[r*D+i])-m; q+=d*d;}
  for(int o=32;o;o>>=1) q+=__shfl_xor(q,o);
  float v=rb(q/D); float mb=rb(m); float sd=rb(sqrtf(rb(v+1e-5f)));
  for(int i=t;i<D;i+=64) xn[r*D+i]=f2b(rb(b2f(x[r*D+i])-mb)/sd);
}
__global__ void k_gemm(const u16* A,const u16* B,u16* C,int M,int N,int K){
  int n=blockIdx.x*64+threadIdx.x; int m=blockIdx.y; if(n>=N) return;
  float a=0; for(int k=0;k<K;++k) a+=b2f(A[m*K+k])*b2f(B[k*N+n]);
  C[m*N+n]=f2b(a);
}
__global__ void k_phi(const u16* qkv,const u16* wphi,u16* phi){
  int idx=blockIdx.x*64+threadIdx.x;
  int s=idx>>6, w=(idx>>5)&1, h=idx&31;
  float p[DH];
  for(int f=0;f<DH;++f){ float a=0; for(int d=0;d<DH;++d) a+=b2f(qkv[s*3*D+w*D+h*DH+d])*b2f(wphi[(h*DH+d)*DH+f]); p[f]=rb(a);}
  float mx=p[0],mn=p[0]; for(int f=1;f<DH;++f){mx=fmaxf(mx,p[f]);mn=fminf(mn,p[f]);}
  float s1=0,s2=0; for(int f=0;f<DH;++f){s1+=expf(p[f]-mx); s2+=expf(mn-p[f]);}
  for(int f=0;f<DH;++f){ phi[(size_t)idx*40+f]=f2b(expf(p[f]-mx)/s1); phi[(size_t)idx*40+20+f]=f2b(expf(mn-p[f])/s2);}
}
__global__ void k_kv(const u16* qkv,const u16* phi,float* kv){
  int idx=blockIdx.x*64+threadIdx.x; if(idx>=H*40*21) return;
  int h=idx/(840), f=(idx/21)%40, d=idx%21;
  float a=0; for(int s=0;s<S;++s){ float pk=b2f(phi[((size_t)s*64+32+h)*40+f]); float v=d<20? b2f(qkv[s*3*D+2*D+h*DH+d]):1.f; a+=pk*v; }
  kv[idx]=rb(a);
}
__global__ void k_o(const u16* phi,const float* kv,u16* o){
  int idx=blockIdx.x*64+threadIdx.x;
  int s=idx/D, c=idx%D, h=c/DH, d=c%DH;
  float num=0,den=0; for(int f=0;f<40;++f){ float pq=b2f(phi[((size_t)s*64+h)*40+f]); num+=pq*kv[h*840+f*21+d]; den+=pq*kv[h*840+f*21+20]; }
  num=rb(num); den=rb(rb(den)+1e-6f); o[idx]=f2b(num/den);
}
__global__ void k_act(u16* h,const u16* b1){
  int idx=blockIdx.x*64+threadIdx.x; float v=rb(b2f(h[idx])+b2f(b1[idx%F])); v=v>0?v:0; h[idx]=f2b(v*v);
}
__global__ void k_fin(const u16* x,const u16* a,const u16* b,const u16* b2,u16* out){
  int idx=blockIdx.x*64+threadIdx.x; float bb=rb(b2f(b[idx])+b2f(b2[idx%D])); float t=rb(b2f(x[idx])+b2f(a[idx])); out[idx]=f2b(t+bb);
}
extern "C" void kernel_launch(void* const* d_in, const int* in_sizes, int n_in, void* d_out, int out_size, void* d_ws, size_t ws_size, hipStream_t stream){
  const u16* x=(const u16*)d_in[0]; const u16* wqkv=(const u16*)d_in[1]; const u16* wphi=(const u16*)d_in[2]; const u16* wo=(const u16*)d_in[3];
  const u16* w1=(const u16*)d_in[5]; const u16* b1=(const u16*)d_in[6]; const u16* w2=(const u16*)d_in[7]; const u16* b2=(const u16*)d_in[8];
  char* ws=(char*)d_ws;
  u16* xn=(u16*)ws; u16* qkv=(u16*)(ws+(4<<20)); u16* phi=(u16*)(ws+(16<<20)); float* kv=(float*)(ws+(32<<20)); u16* o=(u16*)(ws+(36<<20));
  u16* h=(u16*)(ws+(40<<20)); u16* a=(u16*)(ws+(52<<20)); u16* b=(u16*)(ws+(56<<20));
  k_ln<<<S,64,0,stream>>>(x,xn);
  k_gemm<<<dim3(30,S),64,0,stream>>>(xn,wqkv,qkv,S,3*D,D);
  k_phi<<<S,64,0,stream>>>(qkv,wphi,phi);
  k_kv<<<(H*840+63)/64,64,0,stream>>>(qkv,phi,kv);
  k_o<<<S*D/64,64,0,stream>>>(phi,kv,o);
  k_gemm<<<dim3(10,S),64,0,stream>>>(o,wo,a,S,D,D);
  k_gemm<<<dim3(27,S),64,0,stream>>>(xn,w1,h,S,F,D);
  k_act<<<S*F/64,64,0,stream>>>(h,b1);
  k_gemm<<<dim3(10,S),64,0,stream>>>(h,w2,b,S,D,F);
  k_fin<<<S*D/64,64,0,stream>>>(x,a,b,b2,(u16*)d_out);
}
```

```cpp
#include <hip/hip_runtime.h>
#include <hip/hip_bf16.h>
#include <stdint.h>

#define GAS __attribute__((address_space(1)))
#define LAS __attribute__((address_space(3)))
#define DI __device__ __forceinline__
typedef unsigned short u16;
typedef unsigned v4u __attribute__((ext_vector_type(4)));
typedef unsigned v2u __attribute__((ext_vector_type(2)));
typedef float f32x4 __attribute__((ext_vector_type(4)));
typedef float f32x2 __attribute__((ext_vector_type(2)));
typedef short s16x4 __attribute__((ext_vector_type(4)));
typedef short s16x8 __attribute__((ext_vector_type(8)));
typedef __bf16 bf16x2 __attribute__((ext_vector_type(2)));

namespace hk {
constexpr int S = 2048, D = 640, F = 1728, NKS = 74;
constexpr size_t WS_FLAG = 0;
constexpr size_t WS_AFR = 4096;
constexpr size_t WS_WT = WS_AFR + (size_t)128 * NKS * 1024;
constexpr size_t WS_PHQ = WS_WT + (size_t)40 * NKS * 1024;
constexpr size_t WS_KVP = WS_PHQ + (size_t)128 * 32 * 1280;
constexpr size_t WS_STAMP = WS_KVP + (size_t)32 * 16 * 6144;
constexpr int XB = 16384, WP = 480, WB = 64 * WP, BUF = XB + WB;
constexpr int L_WPHI = 3 * BUF, L_ZERO = L_WPHI + 3328, L_CS = L_ZERO + 1024, L_B1 = L_CS + 2048, L1_BYTES = L_B1 + 512;
constexpr int PK = 352, PV = 224;
constexpr int L_PHK = 0, L_V = 2 * BUF, L_PT = BUF;
static_assert(128 * PK + 64 <= BUF && 128 * PV + 64 <= BUF && L1_BYTES <= 163840, "lds");

DI f32x4 mfma32(v4u a, v4u b, f32x4 c) { return __builtin_amdgcn_mfma_f32_16x16x32_bf16(__builtin_bit_cast(s16x8, a), __builtin_bit_cast(s16x8, b), c, 0, 0, 0); }
DI f32x4 mfma16(v2u a, v2u b, f32x4 c) { return mfma32(v4u{a.x, a.y, 0u, 0u}, v4u{b.x, b.y, 0u, 0u}, c); }
DI v2u trd(LAS char* p) { return __builtin_bit_cast(v2u, __builtin_amdgcn_ds_read_tr16_b64_v4i16((LAS s16x4*)p)); }
DI v4u trd2(LAS char* p, int d) { v2u a = trd(p), b = trd(p + d); return v4u{a.x, a.y, b.x, b.y}; }
DI unsigned pk(float a, float b) { f32x2 v = {a, b}; return __builtin_bit_cast(unsigned, __builtin_convertvector(v, bf16x2)); }
DI float lo(unsigned u) { return __builtin_bit_cast(float, u << 16); }
DI float hi(unsigned u) { return __builtin_bit_cast(float, u & 0xffff0000u); }
DI float rb(float a) { return lo(pk(a, a)); }
DI void glds(const void* g, LAS char* l) { __builtin_amdgcn_global_load_lds((const GAS void*)g, (LAS void*)l, 16, 0, 0); }
#define BAR() __builtin_amdgcn_s_barrier()
#define WAITV(n) asm volatile("s_waitcnt vmcnt(" #n ")" ::: "memory")
#define WAITL0() asm volatile("s_waitcnt lgkmcnt(0)" ::: "memory")
#define SB() __builtin_amdgcn_sched_barrier(0)
#define STAMP(i)

struct P {
  const u16 *x, *wqkv, *wphi, *wo, *w1, *b1, *w2, *b2;
  u16* out;
  char* ws;
};

DI void dual_softmax(const f32x4 (&a)[5], unsigned (&w)[20]) {
  float p[20];
#pragma unroll
  for (int t = 0; t < 5; ++t) {
    unsigned u0 = pk(a[t][0], a[t][1]), u1 = pk(a[t][2], a[t][3]);
    p[4 * t] = lo(u0); p[4 * t + 1] = hi(u0); p[4 * t + 2] = lo(u1); p[4 * t + 3] = hi(u1);
  }
  float mx = p[0], mn = p[0];
#pragma unroll
  for (int i = 1; i < 20; ++i) { mx = __builtin_fmaxf(mx, p[i]); mn = __builtin_fminf(mn, p[i]); }
  const float L2E = 1.4426950408889634f;
  float c1 = -mx * L2E, c2 = mn * L2E, s1 = 0.f, s2 = 0.f, e1[20], e2[20];
#pragma unroll
  for (int i = 0; i < 20; ++i) {
    e1[i] = __builtin_amdgcn_exp2f(__builtin_fmaf(p[i], L2E, c1)); s1 += e1[i];
    e2[i] = __builtin_amdgcn_exp2f(__builtin_fmaf(p[i], -L2E, c2)); s2 += e2[i];
  }
  float r1 = __builtin_amdgcn_rcpf(s1), r2 = __builtin_amdgcn_rcpf(s2);
#pragma unroll
  for (int i = 0; i < 10; ++i) { w[i] = pk(e1[2 * i] * r1, e1[2 * i + 1] * r1); w[10 + i] = pk(e2[2 * i] * r2, e2[2 * i + 1] * r2); }
}

template <bool KV>
DI void k1_compute(const P& p, LAS char* L, int panel, int cg, int w, int lane) {
  constexpr int KID = 0;
  constexpr int NT = KV ? 14 : 15;
  const int g = lane >> 4, tok = lane & 15;
  f32x4 acc[2][NT], gram[2], rsum[2];
#pragma unroll
  for (int a = 0; a < 2; ++a) {
    gram[a] = f32x4{0, 0, 0, 0}; rsum[a] = f32x4{0, 0, 0, 0};
#pragma unroll
    for (int t = 0; t < NT; ++t) acc[a][t] = f32x4{0, 0, 0, 0};
  }
  const v4u ones = {0x3f803f80u, 0x3f803f80u, 0x3f803f80u, 0x3f803f80u};
  const int offN = XB + (lane >> 2) * WP + 8 * (lane & 3), offP = XB + (lane >> 2) * WP + 40 * (lane & 3);
  const int xr = w * 32 + tok;
  const int offX0 = xr * 128 + 16 * ((g) ^ ((xr >> 1) & 7)), offX1 = xr * 128 + 16 * ((4 + g) ^ ((xr >> 1) & 7));
  v4u wf[2][5], xf[2][2];
  auto rd = [&](LAS char* B, int n, v4u(&f)[5]) {
    const int s = n / 3, r = n % 3;
    const bool perm = (r == 0) || (KV && r == 1);
#pragma unroll
    for (int i = 0; i < 5; ++i)
      if (5 * r + i < NT) f[i] = perm ? trd2(B + offP + s * 32 * WP + r * 160 + i * 8, 16 * WP) : trd2(B + offN + s * 32 * WP + (5 * r + i) * 32, 16 * WP);
  };
  auto rdx = [&](LAS char* B, int s, v4u(&f)[2]) {
    f[0] = *(LAS v4u*)(B + (s ? offX1 : offX0)); f[1] = *(LAS v4u*)(B + (s ? offX1 : offX0) + 2048);
  };
  auto mm = [&](int n, const v4u(&f)[5], const v4u(&x)[2]) {
    const int r = n % 3;
#pragma unroll
    for (int i = 0; i < 5; ++i)
      if (5 * r + i < NT) { acc[0][5 * r + i] = mfma32(f[i], x[0], acc[0][5 * r + i]); acc[1][5 * r + i] = mfma32(f[i], x[1], acc[1][5 * r + i]); }
    if (r == 1) { gram[0] = mfma32(x[0], x[0], gram[0]); gram[1] = mfma32(x[1], x[1], gram[1]); }
    if (r == 2) { rsum[0] = mfma32(ones, x[0], rsum[0]); rsum[1] = mfma32(ones, x[1], rsum[1]); }
  };
#define ILV(nm) do { _Pragma("unroll") for (int q_ = 0; q_ < (nm); ++q_) { __builtin_amdgcn_sched_group_barrier(0x8, 1, 0); __builtin_amdgcn_sched_group_barrier(0x100, 1, 0); } } while (0)
  BAR();
  STAMP(1);
  int bo = 0;
  rdx(L, 0, xf[0]); rd(L, 0, wf[0]);
  SB();
  for (int s = 0; s < 10; ++s) {
    LAS char* B = L + bo;
    rd(B, 1, wf[1]); mm(0, wf[0], xf[0]); ILV(12); SB();
    rd(B, 2, wf[0]); mm(1, wf[1], xf[0]); ILV(12); SB();
    rdx(B, 1, xf[1]); rd(B, 3, wf[1]); mm(2, wf[0], xf[0]); ILV(12); SB();
    rd(B, 4, wf[0]); mm(3, wf[1], xf[1]); ILV(12); SB();
    rd(B, 5, wf[1]); mm(4, wf[0], xf[1]); ILV(12); SB();
    WAITL0(); BAR(); SB();
    bo = bo == 2 * BUF ? 0 : bo + BUF;
    B = L + bo;
    rdx(B, 0, xf[0]); rd(B, 0, wf[0]); mm(5, wf[1], xf[1]); ILV(12); SB();
  }
  STAMP(2);
  WAITL0();
  float mb[2], rs[2];
#pragma unroll
  for (int a = 0; a < 2; ++a) {
    float d = (lane & 2) ? ((lane & 1) ? gram[a][3] : gram[a][2]) : ((lane & 1) ? gram[a][1] : gram[a][0]);
    float s2 = __builtin_bit_cast(float, __builtin_amdgcn_ds_bpermute(((tok >> 2) * 16 + tok) * 4, __builtin_bit_cast(int, d)));
    float mean = rsum[a][0] * (1.f / D);
    float var = __builtin_fmaxf(s2 * (1.f / D) - mean * mean, 0.f);
    mb[a] = rb(mean);
    rs[a] = 1.f / rb(__builtin_sqrtf(rb(rb(var) + 1e-5f)));
  }
  auto fix = [&](int t, int col) {
    f32x4 c = *(LAS f32x4*)(L + L_CS + col * 4) + *(LAS f32x4*)(L + L_CS + 1024 + col * 4);
#pragma unroll
    for (int a = 0; a < 2; ++a) acc[a][t] = (acc[a][t] - mb[a] * c) * rs[a];
  };
  {
#pragma unroll
    for (int t = 0; t < 5; ++t) fix(t, 20 * g + 4 * t);
    LAS char* wp = L + (((lane & 3) == g) ? L_WPHI + g * 800 + ((lane & 15) >> 2) * 40 : L_ZERO);
    f32x4 pq[2][5];
    v4u bq[2][3];
#pragma unroll
    for (int a = 0; a < 2; ++a) {
      bq[a][0] = v4u{pk(acc[a][0][0], acc[a][0][1]), pk(acc[a][0][2], acc[a][0][3]), pk(acc[a][1][0], acc[a][1][1]), pk(acc[a][1][2], acc[a][1][3])};
      bq[a][1] = v4u{pk(acc[a][2][0], acc[a][2][1]), pk(acc[a][2][2], acc[a][2][3]), pk(acc[a][3][0], acc[a][3][1]), pk(acc[a][3][2], acc[a][3][3])};
      bq[a][2] = v4u{pk(acc[a][4][0], acc[a][4][1]), pk(acc[a][4][2], acc[a][4][3]), 0u, 0u};
    }
#pragma unroll
    for (int t = 0; t < 5; ++t) {
      v4u a0 = trd2(wp + t * 8, 160), a1 = trd2(wp + 320 + t * 8, 160);
      v2u a2 = trd(wp + 640 + t * 8);
#pragma unroll
      for (int a = 0; a < 2; ++a) {
        f32x4 c = mfma32(a0, bq[a][0], f32x4{0, 0, 0, 0});
        c = mfma32(a1, bq[a][1], c);
        pq[a][t] = mfma16(a2, v2u{bq[a][2].x, bq[a][2].y}, c);
      }
    }
#pragma unroll
    for (int a = 0; a < 2; ++a) {
      unsigned wv[20];
      dual_softmax(pq[a], wv);
      if (KV) {
        LAS char* d = L + L_PHK + (w * 32 + a * 16 + tok) * PK + g * 80;
        *(LAS v4u*)(d) = v4u{wv[0], wv[1], wv[4], wv[5]};
        *(LAS v4u*)(d + 16) = v4u{wv[8], wv[9], wv[12], wv[13]};
        *(LAS v4u*)(d + 32) = v4u{wv[2], wv[3], wv[6], wv[7]};
        *(LAS v4u*)(d + 48) = v4u{wv[10], wv[11], wv[14], wv[15]};
        *(LAS v4u*)(d + 64) = v4u{wv[16], wv[17], wv[18], wv[19]};
      } else {
        char* d = p.ws + WS_PHQ + (size_t)((panel * 8 + w * 2 + a) * 32 + (cg - 8) * 4 + g) * 1280 + tok * 16;
#pragma unroll
        for (int i = 0; i < 5; ++i) *(v4u*)(d + i * 256) = v4u{wv[4 * i], wv[4 * i + 1], wv[4 * i + 2], wv[4 * i + 3]};
      }
    }
  }
  if (KV) {
#pragma unroll
    for (int t = 0; t < 5; ++t) fix(5 + t, 80 + 20 * g + 4 * t);
#pragma unroll
    for (int a = 0; a < 2; ++a) {
      LAS char* d = L + L_V + (w * 32 + a * 16 + tok) * PV + g * 48;
      *(LAS v4u*)(d) = v4u{pk(acc[a][5][0], acc[a][5][1]), pk(acc[a][5][2], acc[a][5][3]), pk(acc[a][6][0], acc[a][6][1]), pk(acc[a][6][2], acc[a][6][3])};
      *(LAS v4u*)(d + 16) = v4u{pk(acc[a][7][0], acc[a][7][1]), pk(acc[a][7][2], acc[a][7][3]), pk(acc[a][8][0], acc[a][8][1]), pk(acc[a][8][2], acc[a][8][3])};
      *(LAS v4u*)(d + 32) = v4u{pk(acc[a][9][0], acc[a][9][1]), pk(acc[a][9][2], acc[a][9][3]), 0x00003f80u, 0u};
    }
    WAITL0(); BAR();
  }
  STAMP(3);
  {
    constexpr int T0 = KV ? 10 : 5, NU = (NT - T0) / 2;
    const int j = cg - 8;
    const int ks0 = KV ? 2 * cg : (j < 7 ? 16 + 5 * j : 50);
    const int nu = KV ? 2 : (j < 6 ? 5 : 4);
#pragma unroll
    for (int u = 0; u < NU; ++u) {
      v4u o[2];
#pragma unroll
      for (int e = 0; e < 2; ++e) {
        const int t = T0 + 2 * u + e;
        fix(t, 16 * t + 4 * g);
        v2u bb = *(LAS v2u*)(L + L_B1 + (16 * (t - T0) + 4 * g) * 2);
        float b0 = lo(bb.x), b1 = hi(bb.x), b2 = lo(bb.y), b3 = hi(bb.y);
#pragma unroll
        for (int a = 0; a < 2; ++a) {
          unsigned u0 = pk(acc[a][t][0], acc[a][t][1]), u1 = pk(acc[a][t][2], acc[a][t][3]);
          u0 = pk(lo(u0) + b0, hi(u0) + b1); u1 = pk(lo(u1) + b2, hi(u1) + b3);
          float h0 = __builtin_fmaxf(lo(u0), 0.f), h1 = __builtin_fmaxf(hi(u0), 0.f), h2 = __builtin_fmaxf(lo(u1), 0.f), h3 = __builtin_fmaxf(hi(u1), 0.f);
          o[a][2 * e] = pk(h0 * h0, h1 * h1); o[a][2 * e + 1] = pk(h2 * h2, h3 * h3);
        }
      }
      if (u < nu) {
#pragma unroll
        for (int a = 0; a < 2; ++a) *(v4u*)(p.ws + WS_AFR + (size_t)((panel * 8 + w * 2 + a) * NKS + ks0 + u) * 1024 + lane * 16) = o[a];
      }
    }
  }
  STAMP(4);
}

DI void k1_helper(const P& p, LAS char* L, int panel, int cg, int hw, int lane, int lin) {
  constexpr int KID = 0;
  const bool KV = cg < 8;
  const int j = cg - 8;
  const int head0 = 4 * (cg & 7);
  const int w1c = KV ? 64 * cg : (j < 7 ? 512 + 160 * j : 1600);
  const int cmax = KV ? 27 : (j < 6 ? 29 : 25);
  if (lane < 50) glds((const char*)p.wphi + (head0 + hw) * 800 + lane * 16, L + L_WPHI + hw * 800);
  if (hw == 0 && lane <= cmax - (KV ? 20 : 10)) glds((const char*)p.b1 + w1c * 2 + lane * 16, L + L_B1);
  if (hw == 1) *(LAS v4u*)(L + L_ZERO + lane * 16) = v4u{0, 0, 0, 0};
  const char* wptr[8]; unsigned wadv[8];
#pragma unroll
  for (int i = 0; i < 8; ++i) {
    int n = (hw + 4 * i) * 64 + lane, rho = n / 30, c = n - rho * 30;
    c = c > cmax ? cmax : c;
    int k = (rho & 32) + ((rho >> 2) & 3) * 8 + ((rho >> 4) & 1) * 4 + (rho & 3);
    const int cw = KV ? 20 : 10;
    if (c >= cw) { wptr[i] = (const char*)p.w1 + k * (F * 2) + w1c * 2 + (c - cw) * 16; wadv[i] = 64 * F * 2; }
    else { int col = KV ? (c < 10 ? 640 + 80 * cg + c * 8 : 1280 + 80 * cg + (c - 10) * 8) : 80 * j + c * 8; wptr[i] = (const char*)p.wqkv + k * (3 * D * 2) + col * 2; wadv[i] = 64 * 3 * D * 2; }
  }
  const char* xb = (const char*)p.x + (size_t)(panel * 128 + hw * 32) * (D * 2);
  const unsigned xo0 = (lane >> 3) * (D * 2) + 16 * ((lane & 7) ^ (lane >> 4)), xo1 = (8 + (lane >> 3)) * (D * 2) + 16 * ((lane & 7) ^ (4 | (lane >> 4)));
  auto issue = [&](int s, int bo) {
    LAS char* B = L + bo;
    glds(xb + s * 128 + xo0, B + hw * 4096); glds(xb + s * 128 + xo1, B + hw * 4096 + 1024);
    glds(xb + s * 128 + 16 * D * 2 + xo0, B + hw * 4096 + 2048); glds(xb + s * 128 + 16 * D * 2 + xo1, B + hw * 4096 + 3072);
#pragma unroll
    for (int i = 0; i < 7; ++i) { glds(wptr[i], B + XB + (hw + 4 * i) * 1024); wptr[i] += wadv[i]; }
    if (hw < 2) { glds(wptr[7], B + XB + (hw + 28) * 1024); wptr[7] += wadv[7]; }
  };
  issue(0, 0); issue(1, BUF);
  f32x4 cs[8];
#pragma unroll
  for (int t = 0; t < 8; ++t) cs[t] = f32x4{0, 0, 0, 0};
  const v4u ones = {0x3f803f80u, 0x3f803f80u, 0x3f803f80u, 0x3f803f80u};
  const int offC = XB + (lane >> 2) * WP + 8 * (lane & 3) + (hw & 1) * 32 * WP + (hw >> 1) * 256;
  const int unit = lin * 4 + hw, pks = unit / 10, pcb = unit - pks * 10;
  WAITV(11); WAITL0(); BAR();
  STAMP(1);
  int bo = 0, bn = 2 * BUF;
  for (int s = 0; s < 10; ++s) {
    if (s < 8) issue(s + 2, bn);
    else if (s == 8 && unit < 740) {
#pragma unroll
      for (int i = 0; i < 4; ++i) {
        int rho = 8 * i + (lane >> 3);
        const char* src = pks < 54 ? (const char*)p.w2 + (size_t)(32 * pks + rho) * (D * 2)
                                   : (const char*)p.wo + (size_t)(32 * (pks - 54) + ((rho >> 2) & 3) * 8 + (rho >> 4) * 4 + (rho & 3)) * (D * 2);
        glds(src + pcb * 128 + (lane & 7) * 16, L + L_PT + hw * 4096 + i * 1024);
      }
    }
    SB();
    LAS char* B = L + bo;
    v4u f[8];
#pragma unroll
    for (int t = 0; t < 8; ++t) {
      v2u a, b;
      asm volatile("ds_read_b64_tr_b16 %0, %2 offset:%3\n\tds_read_b64_tr_b16 %1, %2 offset:%4" : "=&v"(a), "=&v"(b) : "v"((unsigned)(size_t)(B + offC)), "n"(t * 32), "n"(t * 32 + 16 * WP) : "memory");
      f[t] = v4u{a.x, a.y, b.x, b.y};
    }
    asm volatile("s_waitcnt lgkmcnt(0)" : "+v"(f[0]), "+v"(f[1]), "+v"(f[2]), "+v"(f[3]), "+v"(f[4]), "+v"(f[5]), "+v"(f[6]), "+v"(f[7])::"memory");
#pragma unroll
    for (int t = 0; t < 8; ++t) cs[t] = mfma32(f[t], ones, cs[t]);
    if (s < 8) WAITV(11); else if (s == 8) { if (unit < 740) WAITV(4); else WAITV(0); }
    if (s == 9 && (lane & 15) == 0) {
#pragma unroll
      for (int t = 0; t < 8; ++t) *(LAS f32x4*)(L + L_CS + (hw & 1) * 1024 + ((hw >> 1) * 128 + 16 * t + 4 * (lane >> 4)) * 4) = cs[t];
    }
    WAITL0(); BAR();
    bn = bo; bo = bo == 2 * BUF ? 0 : bo + BUF;
  }
  STAMP(2);
  WAITV(0);
  if (unit < 740) {
    LAS char* T = L + L_PT + hw * 4096 + (lane >> 2) * 128 + (lane & 3) * 8;
#pragma unroll
    for (int t = 0; t < 4; ++t) {
      v4u f = trd2(T + t * 32, 16 * 128);
      *(v4u*)(p.ws + WS_WT + (size_t)((4 * pcb + t) * NKS + pks) * 1024 + lane * 16) = f;
    }
  }
  if (lin == 0 && hw == 0 && lane < 8) *(unsigned*)(p.ws + WS_FLAG + lane * 256) = 0u;
  if (KV) {
    BAR();
    STAMP(3);
    f32x4 kv[3][2];
#pragma unroll
    for (int T = 0; T < 3; ++T) { kv[T][0] = f32x4{0, 0, 0, 0}; kv[T][1] = f32x4{0, 0, 0, 0}; }
    LAS char* A = L + L_PHK + (lane >> 2) * PK + hw * 80 + (lane & 3) * 8;
    LAS char* Bv = L + L_V + (lane >> 2) * PV + hw * 48 + (lane & 3) * 8;
#pragma unroll
    for (int s = 0; s < 4; ++s) {
      v4u b0 = trd2(Bv + s * 32 * PV, 16 * PV), b1 = trd2(Bv + s * 32 * PV + 32, 16 * PV);
#pragma unroll
      for (int T = 0; T < 3; ++T) {
        v4u a = trd2(A + s * 32 * PK + T * 32, 16 * PK);
        kv[T][0] = mfma32(a, b0, kv[T][0]); kv[T][1] = mfma32(a, b1, kv[T][1]);
      }
    }
    char* d = p.ws + WS_KVP + (size_t)((head0 + hw) * 16 + panel) * 6144 + lane * 16;
#pragma unroll
    for (int T = 0; T < 3; ++T) {
      if (T < 2 || lane < 32) {
        *(f32x4*)(d + T * 2048) = kv[T][0];
        if ((lane & 15) < 8) *(f32x4*)(d + T * 2048 + 1024) = kv[T][1];
      }
    }
  }
  STAMP(4);
}

#define K1MAP 1
__global__ __launch_bounds__(512, 2) void k1(P p) {
  constexpr int KID = 0;
  extern __shared__ __attribute__((aligned(16))) char smem[];
  LAS char* L = (LAS char*)smem;
  STAMP(0);
  const int b = blockIdx.x, xcd = b & 7, r = b >> 3;
  int panel, cg;
  if (K1MAP == 0) { panel = xcd * 2 + (r >> 4); cg = r & 15; }
  else if (K1MAP == 1) { panel = (xcd >> 1) * 4 + (r >> 3); cg = (r & 3) + (xcd & 1) * 4 + ((r >> 2) & 1) * 8; }
  else { panel = (xcd >> 2) * 8 + (r >> 2); cg = (r & 1) + (xcd & 3) * 2 + ((r >> 1) & 1) * 8; }
  const int lane = threadIdx.x & 63, w = __builtin_amdgcn_readfirstlane(threadIdx.x >> 6);
  if (w >= 4) k1_helper(p, L, panel, cg, w - 4, lane, b);
  else if (cg < 8) k1_compute<true>(p, L, panel, cg, w, lane);
  else k1_compute<false>(p, L, panel, cg, w, lane);
}

constexpr int RP = 336, SLOT = 64 * RP;
constexpr int L2_KV = 4 * SLOT, L2_CNT = L2_KV + 4 * 6144, L2_BYTES = L2_CNT + 64;
#define K2MAP 1
#define DEPTH 3
__global__ __launch_bounds__(512, 2) void k2(P p) {
  constexpr int KID = 1;
  extern __shared__ __attribute__((aligned(16))) char smem[];
  LAS char* L = (LAS char*)smem;
  STAMP(0);
  const int b = blockIdx.x, xcd = b & 7, r = b >> 3;
  int rt, ct;
  if (K2MAP == 0) { rt = xcd * 4 + (r >> 3); ct = r & 7; }
  else { rt = (xcd >> 1) * 8 + (r >> 2); ct = (xcd & 1) * 4 + (r & 3); }
  const int tid = threadIdx.x, lane = tid & 63, w = __builtin_amdgcn_readfirstlane(tid >> 6);
  const int g = lane >> 4, tok = lane & 15;
  const char* afr = p.ws + WS_AFR + (size_t)(rt * 4) * NKS * 1024 + lane * 16;
  const char* wt = p.ws + WS_WT + (size_t)(ct * 5) * NKS * 1024 + lane * 16;
  f32x4 acc[4][5];
#pragma unroll
  for (int i = 0; i < 4; ++i)
#pragma unroll
    for (int j = 0; j < 5; ++j) acc[i][j] = f32x4{0, 0, 0, 0};
  LAS char* myslot = L + (w & 3) * SLOT + tok * RP + g * 16;
  if (w < 4) {
    BAR();
    constexpr int NK = 13;
    const char* ap = afr + w * NK * 1024; const char* bp = wt + w * NK * 1024;
    v4u ra[DEPTH][4], rw[DEPTH][5];
#pragma unroll
    for (int d = 0; d < DEPTH; ++d) {
#pragma unroll
      for (int i = 0; i < 4; ++i) ra[d][i] = *(const v4u*)(ap + (i * NKS + d) * 1024);
#pragma unroll
      for (int j = 0; j < 5; ++j) rw[d][j] = *(const v4u*)(bp + (j * NKS + d) * 1024);
    }
    SB();
#pragma unroll
    for (int k = 0; k < NK; ++k) {
      const int d = k % DEPTH;
#pragma unroll
      for (int i = 0; i < 4; ++i)
#pragma unroll
        for (int j = 0; j < 5; ++j) acc[i][j] = mfma32(rw[d][j], ra[d][i], acc[i][j]);
      SB();
      if (k + DEPTH < NK) {
#pragma unroll
        for (int i = 0; i < 4; ++i) ra[d][i] = *(const v4u*)(ap + (i * NKS + k + DEPTH) * 1024);
#pragma unroll
        for (int j = 0; j < 5; ++j) rw[d][j] = *(const v4u*)(bp + (j * NKS + k + DEPTH) * 1024);
      }
      SB();
    }
    STAMP(3);
    BAR();
#pragma unroll
    for (int i = 0; i < 4; ++i)
#pragma unroll
      for (int j = 0; j < 5; ++j) acc[i][j] += *(LAS f32x4*)(myslot + i * 16 * RP + j * 64);
#pragma unroll
    for (int i = 0; i < 4; ++i)
#pragma unroll
      for (int j = 0; j < 5; ++j) *(LAS f32x4*)(myslot + i * 16 * RP + j * 64) = acc[i][j];
  } else {
    const int aw = w - 4;
    const int head = 4 * xcd + (r & 3), tb = r >> 2;
    LAS unsigned* cnt = (LAS unsigned*)(L + L2_CNT);
    if (tid == 256) { cnt[0] = 0; cnt[1] = 0; }
    f32x4 kp[4][6];
    const char* kb = p.ws + WS_KVP + (size_t)(head * 16 + aw * 4) * 6144 + lane * 16;
#pragma unroll
    for (int q = 0; q < 4; ++q)
#pragma unroll
      for (int t = 0; t < 6; ++t) {
        const bool on = (t < 4 || lane < 32) && (!(t & 1) || tok < 8);
        kp[q][t] = f32x4{0, 0, 0, 0};
        if (on) kp[q][t] = *(const f32x4*)(kb + q * 6144 + t * 1024);
      }
    v4u q0[4]; v2u q1[4];
#pragma unroll
    for (int i = 0; i < 4; ++i) {
      const char* qb = p.ws + WS_PHQ + (size_t)((tb * 16 + aw * 4 + i) * 32 + head) * 1280;
      q0[i] = *(const v4u*)(qb + lane * 16); q1[i] = *(const v2u*)(qb + 1024 + tok * 16 + (g & 1) * 8);
    }
    SB();
    WAITL0(); BAR();
#pragma unroll
    for (int t = 0; t < 6; ++t) *(LAS f32x4*)(L + L2_KV + aw * 6144 + t * 1024 + lane * 16) = (kp[0][t] + kp[1][t]) + (kp[2][t] + kp[3][t]);
    v4u rw[5][5];
#pragma unroll
    for (int k = 0; k < 5; ++k)
#pragma unroll
      for (int j = 0; j < 5; ++j) rw[k][j] = *(const v4u*)(wt + (j * NKS + 54 + aw * 5 + k) * 1024);
    SB();
    WAITL0();
    if (lane == 0) __hip_atomic_fetch_add(cnt, 1u, __ATOMIC_RELAXED, __HIP_MEMORY_SCOPE_WORKGROUP);
    while (__hip_atomic_load(cnt, __ATOMIC_RELAXED, __HIP_MEMORY_SCOPE_WORKGROUP) < 4u) __builtin_amdgcn_s_sleep(1);
    asm volatile("" ::: "memory");
    f32x4 kv[6];
#pragma unroll
    for (int t = 0; t < 6; ++t) {
      LAS char* s = L + L2_KV + t * 1024 + lane * 16;
      kv[t] = (*(LAS f32x4*)(s) + *(LAS f32x4*)(s + 6144)) + (*(LAS f32x4*)(s + 2 * 6144) + *(LAS f32x4*)(s + 3 * 6144));
    }
    v4u A0[2]; v2u A1[2];
#pragma unroll
    for (int dt = 0; dt < 2; ++dt) {
      A0[dt] = v4u{pk(kv[dt][0], kv[dt][1]), pk(kv[dt][2], kv[dt][3]), pk(kv[2 + dt][0], kv[2 + dt][1]), pk(kv[2 + dt][2], kv[2 + dt][3])};
      A1[dt] = v2u{pk(kv[4 + dt][0], kv[4 + dt][1]), pk(kv[4 + dt][2], kv[4 + dt][3])};
    }
    STAMP(1);
    const __amdgpu_buffer_rsrc_t rs = __builtin_amdgcn_make_buffer_rsrc(p.ws + WS_AFR, 0, 128 * NKS * 1024, 0x00020000);
    const int Qa = 5 * head + g, Qb = 5 * head + 4;
    const int oa = (54 + (Qa >> 3)) * 1024 + (((Qa & 7) >> 1) * 16 + tok) * 16 + (Qa & 1) * 8;
    const int ob = (54 + (Qb >> 3)) * 1024 + (((Qb & 7) >> 1) * 16 + tok) * 16 + (Qb & 1) * 8;
#pragma unroll
    for (int i = 0; i < 4; ++i) {
      f32x4 o0 = mfma16(A1[0], q1[i], mfma32(A0[0], q0[i], f32x4{0, 0, 0, 0}));
      f32x4 o1 = mfma16(A1[1], q1[i], mfma32(A0[1], q0[i], f32x4{0, 0, 0, 0}));
      float den = __builtin_bit_cast(float, __builtin_amdgcn_ds_bpermute((16 + tok) * 4, __builtin_bit_cast(int, o1[0])));
      float rd = 1.f / rb(rb(den) + 1e-6f);
      unsigned n0 = pk(o0[0], o0[1]), n1 = pk(o0[2], o0[3]), n2 = pk(o1[0], o1[1]), n3 = pk(o1[2], o1[3]);
      v2u sa = {pk(lo(n0) * rd, hi(n0) * rd), pk(lo(n1) * rd, hi(n1) * rd)}, sb = {pk(lo(n2) * rd, hi(n2) * rd), pk(lo(n3) * rd, hi(n3) * rd)};
      const int tbase = (tb * 16 + aw * 4 + i) * NKS * 1024;
      __builtin_amdgcn_raw_buffer_store_b64(sa, rs, tbase + oa, 0, 16);
      if (g == 0) __builtin_amdgcn_raw_buffer_store_b64(sb, rs, tbase + ob, 0, 16);
    }
    WAITV(0);
    if (lane == 0) {
      unsigned old = __hip_atomic_fetch_add(cnt + 1, 1u, __ATOMIC_RELAXED, __HIP_MEMORY_SCOPE_WORKGROUP);
      if (old == 3u) __hip_atomic_fetch_add((GAS unsigned*)(p.ws + WS_FLAG + tb * 256), 1u, __ATOMIC_RELAXED, __HIP_MEMORY_SCOPE_AGENT);
    }
    STAMP(2);
    if (aw < 2) {
      v4u ea[4], ew[5];
#pragma unroll
      for (int i = 0; i < 4; ++i) ea[i] = *(const v4u*)(afr + (i * NKS + 52 + aw) * 1024);
#pragma unroll
      for (int j = 0; j < 5; ++j) ew[j] = *(const v4u*)(wt + (j * NKS + 52 + aw) * 1024);
#pragma unroll
      for (int i = 0; i < 4; ++i)
#pragma unroll
        for (int j = 0; j < 5; ++j) acc[i][j] = mfma32(ew[j], ea[i], acc[i][j]);
    }
    {
      GAS unsigned* fl = (GAS unsigned*)(p.ws + WS_FLAG + (rt >> 2) * 256);
      for (unsigned spins = 0; spins < (1u << 20); ++spins) {
        if (__builtin_amdgcn_readfirstlane(__hip_atomic_load(fl, __ATOMIC_RELAXED, __HIP_MEMORY_SCOPE_AGENT)) >= 32u) break;
        __builtin_amdgcn_s_sleep(2);
      }
      __builtin_amdgcn_fence(__ATOMIC_ACQUIRE, "wavefront");
    }
    STAMP(3);
    v4u ra[3][4];
    const int abase = (rt * 4 * NKS + 54 + aw * 5) * 1024 + lane * 16;
#pragma unroll
    for (int k = 0; k < 3; ++k)
#pragma unroll
      for (int i = 0; i < 4; ++i) ra[k][i] = __builtin_amdgcn_raw_buffer_load_b128(rs, abase + (i * NKS + k) * 1024, 0, 16);
    SB();
#pragma unroll
    for (int k = 0; k < 5; ++k) {
#pragma unroll
      for (int i = 0; i < 4; ++i)
#pragma unroll
        for (int j = 0; j < 5; ++j) acc[i][j] = mfma32(rw[k][j], ra[k % 3][i], acc[i][j]);
      SB();
      if (k < 2) {
#pragma unroll
        for (int i = 0; i < 4; ++i) ra[k][i] = __builtin_amdgcn_raw_buffer_load_b128(rs, abase + (i * NKS + k + 3) * 1024, 0, 16);
      }
      SB();
    }
#pragma unroll
    for (int i = 0; i < 4; ++i)
#pragma unroll
      for (int j = 0; j < 5; ++j) *(LAS f32x4*)(myslot + i * 16 * RP + j * 64) = acc[i][j];
    STAMP(4);
    WAITL0(); BAR();
  }
  WAITL0(); BAR();
  STAMP(5);
  for (int task = tid; task < 640; task += 512) {
    const int row = task / 10, c = task - row * 10;
    LAS char* s = L + row * RP + c * 32;
    f32x4 a = (*(LAS f32x4*)(s) + *(LAS f32x4*)(s + SLOT)) + (*(LAS f32x4*)(s + 2 * SLOT) + *(LAS f32x4*)(s + 3 * SLOT));
    f32x4 bq = (*(LAS f32x4*)(s + 16) + *(LAS f32x4*)(s + SLOT + 16)) + (*(LAS f32x4*)(s + 2 * SLOT + 16) + *(LAS f32x4*)(s + 3 * SLOT + 16));
    const size_t go = (size_t)(rt * 64 + row) * (D * 2) + ct * 160 + c * 16;
    v4u xv = *(const v4u*)((const char*)p.x + go), bv = *(const v4u*)((const char*)p.b2 + ct * 160 + c * 16);
    v4u o;
    o.x = pk(a[0] + lo(xv.x) + lo(bv.x), a[1] + hi(xv.x) + hi(bv.x)); o.y = pk(a[2] + lo(xv.y) + lo(bv.y), a[3] + hi(xv.y) + hi(bv.y));
    o.z = pk(bq[0] + lo(xv.z) + lo(bv.z), bq[1] + hi(xv.z) + hi(bv.z)); o.w = pk(bq[2] + lo(xv.w) + lo(bv.w), bq[3] + hi(xv.w) + hi(bv.w));
    *(v4u*)((char*)p.out + go) = o;
  }
  STAMP(6);
}
}

extern "C" void kernel_launch(void* const* d_in, const int* in_sizes, int n_in, void* d_out, int out_size, void* d_ws, size_t ws_size, hipStream_t stream) {
  using namespace hk;
  P p;
  p.x = (const u16*)d_in[0]; p.wqkv = (const u16*)d_in[1]; p.wphi = (const u16*)d_in[2]; p.wo = (const u16*)d_in[3];
  p.w1 = (const u16*)d_in[5]; p.b1 = (const u16*)d_in[6]; p.w2 = (const u16*)d_in[7]; p.b2 = (const u16*)d_in[8];
  p.out = (u16*)d_out; p.ws = (char*)d_ws;
  (void)hipFuncSetAttribute((const void*)k1, hipFuncAttributeMaxDynamicSharedMemorySize, L1_BYTES);
  (void)hipFuncSetAttribute((const void*)k2, hipFuncAttributeMaxDynamicSharedMemorySize, L2_BYTES);
  k1<<<256, 512, L1_BYTES, stream>>>(p);
  k2<<<256, 512, L2_BYTES, stream>>>(p);
}
```

```cpp
#include <hip/hip_runtime.h>
#include <hip/hip_bf16.h>
#include <stdint.h>

#define GAS __attribute__((address_space(1)))
#define LAS __attribute__((address_space(3)))
#define DI __device__ __forceinline__
typedef unsigned short u16;
typedef unsigned long long u64;
typedef unsigned v4u __attribute__((ext_vector_type(4)));
typedef unsigned v2u __attribute__((ext_vector_type(2)));
typedef float f32x4 __attribute__((ext_vector_type(4)));
typedef float f32x2 __attribute__((ext_vector_type(2)));
typedef short s16x4 __attribute__((ext_vector_type(4)));
typedef short s16x8 __attribute__((ext_vector_type(8)));
typedef __bf16 bf16x2 __attribute__((ext_vector_type(2)));

namespace hk {
constexpr int S = 2048, D = 640, F = 1728, NKS = 74;
constexpr int NCOL = 3 * D + F;
constexpr unsigned WS_EPOCH = 0;
constexpr unsigned WS_FLAG = 4096;
constexpr unsigned FL_PT = WS_FLAG, FL_H = WS_FLAG + 16384, FL_A = WS_FLAG + 32768, FL_O = WS_FLAG + 49152;
constexpr unsigned WS_RS = WS_FLAG + 65536;
constexpr unsigned WS_CS = WS_RS + S * 8;
constexpr unsigned WS_AFR = 262144;
constexpr unsigned WS_WT = WS_AFR + 128 * NKS * 1024;
constexpr unsigned WS_PHQ = WS_WT + 40 * NKS * 1024;
constexpr unsigned KVB = 3456;
constexpr unsigned WS_KVP = WS_PHQ + 128 * 32 * 1280;
constexpr unsigned WS_END = WS_KVP + 32 * 16 * KVB;
constexpr unsigned WS_STAMP = WS_END + 4096;
static_assert(WS_CS + 4 * NCOL * 8 <= WS_AFR, "ws");
constexpr int XB = 16384, WR = 1056, BUF = XB + 32 * WR;
constexpr int L_WPHI = 3 * BUF, L_ZERO = L_WPHI + 3328, L_CS = L_ZERO + 1024, L_B1 = L_CS + 1024, L_CP = L_B1 + 1024, L_CNT = L_CP + 1280, L_BYTES = L_CNT + 64 + 1024;
constexpr int PK = 352, PV = 224;
constexpr int L_PHK = 0, L_V = 2 * BUF, L_P = L_V + 128 * PV + 64;
constexpr int RP = 336, SLOT = 64 * RP;
constexpr int L_KV = 4 * SLOT;
static_assert(128 * PK + 64 <= BUF && L_P + 4 * 5120 <= 3 * BUF && L_KV + 4 * 6144 <= 3 * BUF && L_BYTES <= 163840, "lds");

DI f32x4 mfma32(v4u a, v4u b, f32x4 c) { return __builtin_amdgcn_mfma_f32_16x16x32_bf16(__builtin_bit_cast(s16x8, a), __builtin_bit_cast(s16x8, b), c, 0, 0, 0); }
DI f32x4 mfma16(v2u a, v2u b, f32x4 c) { return mfma32(v4u{a.x, a.y, 0u, 0u}, v4u{b.x, b.y, 0u, 0u}, c); }
DI v2u trd(LAS char* p) { return __builtin_bit_cast(v2u, __builtin_amdgcn_ds_read_tr16_b64_v4i16((LAS s16x4*)p)); }
DI v4u trd2(LAS char* p, int d) { v2u a = trd(p), b = trd(p + d); return v4u{a.x, a.y, b.x, b.y}; }
DI unsigned pk(float a, float b) { f32x2 v = {a, b}; return __builtin_bit_cast(unsigned, __builtin_convertvector(v, bf16x2)); }
DI v2u pk4(f32x4 a) { return v2u{pk(a[0], a[1]), pk(a[2], a[3])}; }
DI v4u pk8(f32x4 a, f32x4 b) { return v4u{pk(a[0], a[1]), pk(a[2], a[3]), pk(b[0], b[1]), pk(b[2], b[3])}; }
DI float lo(unsigned u) { return __builtin_bit_cast(float, u << 16); }
DI float hi(unsigned u) { return __builtin_bit_cast(float, u & 0xffff0000u); }
DI float rb(float a) { return lo(pk(a, a)); }
DI void glds(const void* g, unsigned dst) { asm volatile("s_mov_b32 m0, %1\n\ts_nop 0\n\tglobal_load_lds_dwordx4 %0, off" ::"v"(g), "s"(dst) : "memory"); }
DI void gldso(const void* base, unsigned off, unsigned dst) { asm volatile("s_mov_b32 m0, %2\n\ts_nop 0\n\tglobal_load_lds_dwordx4 %0, %1" ::"v"(off), "s"(base), "s"(dst) : "memory"); }
#define BAR() __builtin_amdgcn_s_barrier()
#define WAITV(n) asm volatile("s_waitcnt vmcnt(" #n ")" ::: "memory")
#define WAITL0() asm volatile("s_waitcnt lgkmcnt(0)" ::: "memory")
#define SB() __builtin_amdgcn_sched_barrier(0)
typedef __amdgpu_buffer_rsrc_t rsrc_t;
DI v4u ld16(rsrc_t r, unsigned off) { return __builtin_amdgcn_raw_buffer_load_b128(r, off, 0, 16); }
DI v2u ld8(rsrc_t r, unsigned off) { return __builtin_amdgcn_raw_buffer_load_b64(r, off, 0, 16); }
DI unsigned ld4(rsrc_t r, unsigned off) { return __builtin_amdgcn_raw_buffer_load_b32(r, off, 0, 16); }
DI void st16(rsrc_t r, unsigned off, v4u v) { __builtin_amdgcn_raw_buffer_store_b128(v, r, off, 0, 16); }
DI void st8(rsrc_t r, unsigned off, v2u v) { __builtin_amdgcn_raw_buffer_store_b64(v, r, off, 0, 16); }
DI void st4(rsrc_t r, unsigned off, unsigned v) { __builtin_amdgcn_raw_buffer_store_b32(v, r, off, 0, 16); }
constexpr unsigned SPINS = 1u << 16;
DI void wait_flags(rsrc_t r, unsigned off, unsigned tag) {
#pragma unroll 1
  for (unsigned i = 0; i < SPINS; ++i) {
    asm volatile("" ::: "memory");
    if (__builtin_amdgcn_ballot_w64(ld4(r, off) != tag) == 0) break;
    __builtin_amdgcn_s_sleep(1);
  }
  asm volatile("" ::: "memory");
}
template <int CTRL, int RM> DI float dpp_add(float v) { return v + __builtin_bit_cast(float, __builtin_amdgcn_update_dpp(0, __builtin_bit_cast(int, v), CTRL, RM, 0xf, false)); }
DI float wave_sum(float v) {
  v = dpp_add<0x111, 0xf>(v); v = dpp_add<0x112, 0xf>(v); v = dpp_add<0x114, 0xf>(v); v = dpp_add<0x118, 0xf>(v);
  v = dpp_add<0x142, 0xa>(v); v = dpp_add<0x143, 0xc>(v);
  return __builtin_bit_cast(float, __builtin_amdgcn_readlane(__builtin_bit_cast(int, v), 63));
}

struct P {
  const u16 *x, *wqkv, *wphi, *wo, *w1, *b1, *w2, *b2;
  u16* out;
  char* ws;
};

DI void dual_softmax(const f32x4 (&a)[5], unsigned (&w)[20]) {
  float mx = a[0][0], mn = a[0][0];
#pragma unroll
  for (int i = 1; i < 20; ++i) { mx = __builtin_fmaxf(mx, a[i >> 2][i & 3]); mn = __builtin_fminf(mn, a[i >> 2][i & 3]); }
  const float L2E = 1.4426950408889634f;
  float c1 = -mx * L2E, c2 = mn * L2E, s1 = 0.f, s2 = 0.f, e[20];
#pragma unroll
  for (int i = 0; i < 20; ++i) { e[i] = __builtin_amdgcn_exp2f(__builtin_fmaf(a[i >> 2][i & 3], L2E, c1)); s1 += e[i]; }
  float r1 = __builtin_amdgcn_rcpf(s1);
#pragma unroll
  for (int i = 0; i < 10; ++i) w[i] = pk(e[2 * i] * r1, e[2 * i + 1] * r1);
#pragma unroll
  for (int i = 0; i < 20; ++i) { e[i] = __builtin_amdgcn_exp2f(__builtin_fmaf(a[i >> 2][i & 3], -L2E, c2)); s2 += e[i]; }
  float r2 = __builtin_amdgcn_rcpf(s2);
#pragma unroll
  for (int i = 0; i < 10; ++i) w[10 + i] = pk(e[2 * i] * r2, e[2 * i + 1] * r2);
}

struct Ctx {
  LAS char* L; rsrc_t rw; unsigned tag;
  int b, panel, cg, w, lane, rt, ct;
  const char* wp[4]; u64 wadv; const char* xb; unsigned xo0, xo1;
};
DI void piece(Ctx& c, int i, int dx, int dw) {
  if (i < 2) {
    asm volatile("s_add_i32 m0, %2, %3\n\ts_nop 0\n\tglobal_load_lds_dwordx4 %0, %1" ::"v"(i ? c.xo1 : c.xo0), "s"(c.xb), "s"(dx), "n"(i * 1024) : "memory", "scc");
    if (i) c.xb += 128;
  } else {
    asm volatile("s_add_i32 m0, %2, %3\n\tv_lshl_add_u64 %0, %0, 0, %1\n\tglobal_load_lds_dwordx4 %0, off" : "+v"(c.wp[i - 2]) : "v"(c.wadv), "s"(dw), "n"((i - 2) * WR) : "memory", "scc");
  }
}
DI int img_col(int cg, int ch) {
  const int win = ((160 * (cg & 7)) & ~63) >> 1, j = cg - 8;
  const int b0 = cg < 8 ? D + win : win;
  const int b1 = cg < 8 ? 2 * D + win - 96 : 3 * D + (j < 7 ? 512 + 160 * j : 1600) - 96;
  const int b2 = cg < 8 ? 3 * D + 64 * cg - 192 : b1;
  const int cc = j >= 6 && ch > 27 ? 27 : ch;
  return (ch < 12 ? b0 : ch < 24 ? b1 : b2) + 8 * cc;
}

template <int NP, int NN>
DI void gemm1(const P& p, Ctx& c, int pbase, int nbase, f32x4 (&acc)[2][NP + NN], v4u (&wpre)[4][5], float (&mb)[2], float (&rs)[2]) {
  constexpr int NT = NP + NN;
  LAS char* L = c.L;
  const int lane = c.lane, w = c.w, g = lane >> 4, tok = lane & 15;
  const int offN = XB + (lane >> 2) * WR + 8 * (lane & 3) + nbase, offP = XB + (lane >> 2) * WR + 40 * (lane & 3) + pbase;
  const int xr = (w & 3) * 32 + tok;
  const int offX = xr * 128 + 16 * (g ^ ((xr >> 1) & 7));
  v2u wf[NT][2]; v4u xf[2][2];
  LAS char *aN, *aP, *aX0, *aX1;
  auto base = [&](int bo) { aN = L + offN + bo; aP = L + offP + bo; aX0 = L + offX + bo; aX1 = L + (offX ^ 64) + bo; asm volatile("" : "+v"(aN), "+v"(aP), "+v"(aX0), "+v"(aX1)); };
  auto rd = [&](int s, int t, int h) { wf[t][h] = trd((t < NP ? aP + t * 8 : aN + (t - NP) * 32) + s * 512 + h * 16 * WR); };
  auto rdx = [&](int s, int a) { xf[s][a] = *(LAS v4u*)((s ? aX1 : aX0) + a * 2048); };
  auto mm = [&](int s, int t, int a, bool first = false) {
    const v4u f = {wf[t][0].x, wf[t][0].y, wf[t][1].x, wf[t][1].y};
    if (first) asm volatile("v_mfma_f32_16x16x32_bf16 %0, %1, %2, 0" : "=&v"(acc[a][t]) : "v"(f), "v"(xf[s][a]));
    else asm volatile("v_mfma_f32_16x16x32_bf16 %0, %1, %2, %0" : "+v"(acc[a][t]) : "v"(f), "v"(xf[s][a]));
  };
  constexpr int PR1 = 4, PR2 = 8, PR3 = NT + 4;
  auto prio = [&](int n) { if (n == 0) __builtin_amdgcn_s_setprio(3); else if (n == PR1) __builtin_amdgcn_s_setprio(2); else if (n == PR2) __builtin_amdgcn_s_setprio(1); else if (n == PR3) __builtin_amdgcn_s_setprio(0); };
  auto k1b = [&](auto&& hook) {
#pragma unroll
    for (int t = 2; t < NT; ++t) {
      prio(t - 2);
      mm(1, t, 0);
      if (t == 2) { rdx(0, 0); rdx(0, 1); } else if (t == 3) { rd(0, 1, 0); rd(0, 1, 1); } else rd(0, t - 1, 0);
      mm(1, t, 1);
      if (t == 2) { rd(0, 0, 0); rd(0, 0, 1); } else if (t == 3) { rd(0, 2, 0); rd(0, 2, 1); } else rd(0, t - 1, 1);
      hook(t - 2);
    }
  };
  auto k0 = [&](auto&& hook, bool first = false) {
#pragma unroll
    for (int t = 0; t < NT; ++t) {
      prio(NT - 2 + t);
      mm(0, t, 0, first);
      if (t) rd(1, t - 1, 0); else if (!first) rd(0, NT - 1, 0);
      if (t == 1 || t == 2) rdx(1, t - 1);
      mm(0, t, 1, first);
      if (t) rd(1, t - 1, 1); else if (!first) rd(0, NT - 1, 1);
      hook(NT - 2 + t);
    }
    mm(1, 0, 0); rd(1, NT - 1, 0); mm(1, 0, 1); rd(1, NT - 1, 1);
  };
  auto close = [&]() { mm(1, 1, 0); mm(1, 1, 1); WAITL0(); BAR(); };
  int bo = 0, dx = 2 * BUF + w * 2048, dw = 2 * BUF + XB + w * 4 * WR;
  auto ring = [&](int n) { if (n < 12 && !(n & 1)) piece(c, n >> 1, dx, dw); };
  base(0);
  rdx(0, 0); rdx(0, 1);
#pragma unroll
  for (int t = 0; t < NT; ++t) { rd(0, t, 0); rd(0, t, 1); }
  k0([&](int n) { if (n - (NT - 2) < 6) piece(c, n - (NT - 2), dx, dw); }, true);
  {
    LAS float* q = (LAS float*)(L + L_CP) + lane;
    float v = ((q[0] + q[64]) + (q[128] + q[192])) + q[256];
    if (w == 3) st8(c.rw, c.b < 228 ? WS_CS + ((c.b / 57) * NCOL + (c.b % 57) * 64 + lane) * 8 : 0xfffffff0u, v2u{__builtin_bit_cast(unsigned, v), c.tag});
  }
  if (w < 3) WAITV(11); else if (w == 3) WAITV(8); else WAITV(7);
  const unsigned fo = FL_PT + (((5 * c.ct) >> 2) + (lane >> 5)) * 128 + ((lane & 31) < 25 ? lane & 31 : 0) * 4;
  const unsigned po = w == 0 && lane == 0 ? FL_PT + (c.b % 10) * 128 + (c.b / 10) * 4 : 0xfffffff0u;
  unsigned fl = c.tag;
#pragma unroll 1
  for (int s = 0; s < 7; ++s) {
    dx += bo - (bo == 0 ? 2 * BUF : bo - BUF); dw += bo - (bo == 0 ? 2 * BUF : bo - BUF);
    bo = bo == 2 * BUF ? 0 : bo + BUF;
    base(bo);
    close();
    k1b([&](int n) {
      if (n == 0) asm volatile("s_cmp_lg_u32 %4, 2\n\ts_cbranch_scc1 1f\n\tbuffer_store_dword %0, %2, %3, 0 offen sc1\n1:\ts_cmp_lg_u32 %4, 6\n\ts_cbranch_scc1 2f\n\tbuffer_load_dword %0, %1, %3, 0 offen sc1\n2:"
                               : "+v"(fl) : "v"(fo), "v"(po), "s"(c.rw), "s"(s) : "memory", "scc");
      ring(n);
    });
    k0(ring);
    WAITV(6);
  }
  const unsigned wo = WS_WT + (c.ct * 5 * NKS + (w < 4 ? 13 * w : 34 + 5 * w)) * 1024 + lane * 16;
  auto wl = [&](int n) { if (n < 20) { wpre[n / 5][n % 5] = ld16(c.rw, wo + ((n % 5) * NKS + n / 5) * 1024); } };
  asm volatile("" : "+v"(fl));
  if (__builtin_amdgcn_ballot_w64(fl != c.tag)) wait_flags(c.rw, fo, c.tag);
  base(2 * BUF);
  close();
  {
    const int tid = w * 64 + lane;
    const int col = img_col(c.cg, (tid >> 3) & 31) + (tid & 7);
    const unsigned ro = WS_RS + (c.panel * 128 + xr) * 8, co = WS_CS + col * 8;
    v2u r0, r1, c0, c1, c2, c3; unsigned bb;
    auto stat = [&]() {
      asm volatile("buffer_load_dwordx2 %0, %2, %3, 0 offen sc1\n\tbuffer_load_dwordx2 %1, %2, %3, 0 offen offset:128 sc1" : "=&v"(r0), "=&v"(r1) : "v"(ro), "s"(c.rw) : "memory");
      asm volatile("buffer_load_dwordx2 %0, %4, %5, 0 offen sc1\n\tbuffer_load_dwordx2 %1, %4, %5, %6 offen sc1\n\tbuffer_load_dwordx2 %2, %4, %5, %7 offen sc1\n\tbuffer_load_dwordx2 %3, %4, %5, %8 offen sc1"
                   : "=&v"(c0), "=&v"(c1), "=&v"(c2), "=&v"(c3) : "v"(w < 4 ? co : 0xf0000000u), "s"(c.rw), "s"(NCOL * 8), "s"(2 * NCOL * 8), "s"(3 * NCOL * 8) : "memory");
    };
    k1b([&](int n) {
      if (n == 0) { stat(); asm volatile("global_load_ushort %0, %1, %2" : "=v"(bb) : "v"((col >= 3 * D ? col - 3 * D : 0) * 2), "s"(p.b1) : "memory"); }
      else wl(n - 1);
    });
    k0([&](int n) { if (n < 11) wl(n - 1); });
    asm volatile("s_waitcnt vmcnt(10)" : "+v"(r0), "+v"(r1), "+v"(c0), "+v"(c1), "+v"(c2), "+v"(c3), "+v"(bb)::"memory");
    if (__builtin_amdgcn_ballot_w64(r0.y != c.tag || r1.y != c.tag || (w < 4 && (c0.y != c.tag || c1.y != c.tag || c2.y != c.tag || c3.y != c.tag)))) {
#pragma unroll 1
      for (unsigned i = 0; i < SPINS; ++i) {
        stat();
        asm volatile("s_waitcnt vmcnt(0)" : "+v"(r0), "+v"(r1), "+v"(c0), "+v"(c1), "+v"(c2), "+v"(c3)::"memory");
        if (!__builtin_amdgcn_ballot_w64(r0.y != c.tag || r1.y != c.tag || (w < 4 && (c0.y != c.tag || c1.y != c.tag || c2.y != c.tag || c3.y != c.tag)))) break;
      }
    }
    mb[0] = lo(r0.x); rs[0] = __builtin_amdgcn_rcpf(hi(r0.x)); mb[1] = lo(r1.x); rs[1] = __builtin_amdgcn_rcpf(hi(r1.x));
    if (w < 4) {
      *(LAS float*)(L + L_CS + tid * 4) = (__builtin_bit_cast(float, c0.x) + __builtin_bit_cast(float, c1.x)) + (__builtin_bit_cast(float, c2.x) + __builtin_bit_cast(float, c3.x));
      *(LAS float*)(L + L_B1 + tid * 4) = lo(bb);
    }
    base(0);
    close();
  }
  {
    k1b([&](int n) { wl(10 + n); });
    k0([&](int n) { wl(10 + n); });
#pragma unroll
    for (int t = 1; t < NT; ++t) { mm(1, t, 0); mm(1, t, 1); }
    asm volatile("s_nop 15" ::: "memory");
    SB();
  }
}
DI void hidden(Ctx& c, const f32x4& a00, const f32x4& a01, const f32x4& a10, const f32x4& a11, int pos, int ks, bool on, const float (&mb)[2], const float (&rs)[2]) {
  LAS char* t = c.L + pos * 2 + (c.lane >> 4) * 16;
  const f32x4 cs0 = *(LAS f32x4*)(t + L_CS), cs1 = *(LAS f32x4*)(t + L_CS + 64), b0 = *(LAS f32x4*)(t + L_B1), b1 = *(LAS f32x4*)(t + L_B1 + 64);
#pragma unroll
  for (int a = 0; a < 2; ++a) {
    const float n = -mb[a] * rs[a];
    f32x4 u = (a ? a10 : a00) * rs[a] + (cs0 * n + b0), v = (a ? a11 : a01) * rs[a] + (cs1 * n + b1);
#pragma unroll
    for (int i = 0; i < 4; ++i) { u[i] *= __builtin_fmaxf(u[i], 0.f); v[i] *= __builtin_fmaxf(v[i], 0.f); }
    if (on) st16(c.rw, WS_AFR + ((c.panel * 8 + (c.w & 3) * 2 + a) * NKS + ks) * 1024 + c.lane * 16, pk8(u, v));
  }
}

#define K1MAP 1
#define K2MAP 0
__global__ __launch_bounds__(512, 2) void fk(P p) {
  extern __shared__ __attribute__((aligned(16))) char smem[];
  LAS char* L = (LAS char*)smem;
  Ctx c;
  const int b = blockIdx.x, xcd = b & 7, r = b >> 3;
  if (K1MAP == 0) { c.panel = xcd * 2 + (r >> 4); c.cg = r & 15; }
  else if (K1MAP == 1) { c.panel = (xcd >> 1) * 4 + (r >> 3); c.cg = (r & 3) + (xcd & 1) * 4 + ((r >> 2) & 1) * 8; }
  else { c.panel = (xcd >> 2) * 8 + (r >> 2); c.cg = (r & 1) + (xcd & 3) * 2 + ((r >> 1) & 1) * 8; }
  if (K2MAP == 0) { c.rt = xcd * 4 + (r >> 3); c.ct = r & 7; }
  else { c.rt = (xcd >> 1) * 8 + (r >> 2); c.ct = (xcd & 1) * 4 + (r & 3); }
  const int tid = threadIdx.x, lane = tid & 63, w = __builtin_amdgcn_readfirstlane(tid >> 6);
  const int g = lane >> 4, tok = lane & 15, panel = c.panel, cg = c.cg, pid = panel * 16 + cg;
  const bool KV = cg < 8;
  c.L = L; c.b = b; c.w = w; c.lane = lane;
  c.rw = __builtin_amdgcn_make_buffer_rsrc(p.ws, 0, WS_END, 0x00020000);
  unsigned ep; v4u xa, xc;
  {
    const char* row = (const char*)p.x + (size_t)(panel * 128 + cg * 8 + w) * (D * 2);
    asm volatile("global_load_dword %0, %1, %2 sc1" : "=v"(ep) : "v"(0u), "s"(p.ws) : "memory");
    asm volatile("global_load_dwordx4 %0, %1, %2" : "=v"(xa) : "v"(lane * 16), "s"(row) : "memory");
    asm volatile("global_load_dwordx4 %0, %1, %2 offset:1024" : "=v"(xc) : "v"(tok * 16), "s"(row) : "memory");
  }
  const int pks0 = 3 * (b / 10) + w, pks = pks0 < NKS ? pks0 : NKS - 1, pcb = b % 10;
  {
    const char* src; unsigned o0, st;
    if (w < 3) {
      if (pks < 54) { src = (const char*)p.w2 + (size_t)(32 * pks) * (D * 2); o0 = (lane >> 3) * (D * 2); st = 8 * D * 2; }
      else { src = (const char*)p.wo + (size_t)(32 * (pks - 54)) * (D * 2); o0 = ((lane >> 5) * 8 + ((lane >> 3) & 3)) * (D * 2); st = 16 * D * 2; }
      src += pcb * 128;
    } else {
      const int jb = b < 228 ? b : 0, lc = jb % 57, k0 = (jb / 57) * 160 + (w - 3) * 32;
      if (lc < 30) { src = (const char*)p.wqkv + (size_t)k0 * (3 * D * 2) + lc * 128; st = 8 * 3 * D * 2; }
      else { src = (const char*)p.w1 + (size_t)k0 * (F * 2) + (lc - 30) * 128; st = 8 * F * 2; }
      o0 = (lane >> 3) * (st >> 3);
    }
    o0 += (lane & 7) * 16;
    const unsigned st2 = (w < 3 && pks >= 54) ? 4 * D * 2 : 2 * st;
    const unsigned dst = 2 * BUF + w * 4096;
    gldso(src, o0, dst); gldso(src, o0 + st, dst + 1024); gldso(src, o0 + st2, dst + 2048); gldso(src, o0 + st2 + st, dst + 3072);
  }
  {
    const int col = img_col(cg, lane & 31), k = (lane >> 5) * 32 + (w & 3) * 8 + (w >> 2) * 4;
    const bool f1 = col >= 3 * D;
    const unsigned pitch = f1 ? F * 2 : 3 * D * 2;
    const char* q = (f1 ? (const char*)p.w1 - 3 * D * 2 : (const char*)p.wqkv) + (k * pitch + col * 2);
    c.wadv = 64 * pitch;
#pragma unroll
    for (int i = 0; i < 4; ++i) c.wp[i] = q + i * pitch - c.wadv;
    c.xb = (const char*)p.x + (size_t)(panel * 128 + w * 16) * (D * 2);
    c.xo0 = (lane >> 3) * (D * 2) + 16 * ((lane & 7) ^ (lane >> 4)); c.xo1 = (8 + (lane >> 3)) * (D * 2) + 16 * ((lane & 7) ^ (4 | (lane >> 4)));
  }
#pragma unroll
  for (int i = 0; i < 6; ++i) piece(c, i, w * 2048, XB + w * 4 * WR);
  if (lane < 25) gldso((const char*)p.wphi + (cg & 7) * 3200 + w * 400, lane * 16, L_WPHI + w * 400);
#pragma unroll
  for (int i = 0; i < 6; ++i) piece(c, i, BUF + w * 2048, BUF + XB + w * 4 * WR);
  if (w == 7) *(LAS v4u*)(L + L_ZERO + lane * 16) = v4u{0, 0, 0, 0};
  if (tid == 0) { *(LAS unsigned*)(L + L_CNT) = 0; *(LAS unsigned*)(L + L_CNT + 4) = 0; }
  asm volatile("s_waitcnt vmcnt(17)" : "+v"(ep), "+v"(xa), "+v"(xc)::"memory");
  c.tag = __builtin_amdgcn_readfirstlane(ep) + 1u;
  unsigned stat;
  {
    float v[16];
#pragma unroll
    for (int i = 0; i < 4; ++i) { v[2 * i] = lo(xa[i]); v[2 * i + 1] = hi(xa[i]); v[8 + 2 * i] = lo(xc[i]); v[9 + 2 * i] = hi(xc[i]); }
    float s0 = 0.f, s1 = 0.f;
#pragma unroll
    for (int i = 0; i < 8; ++i) { s0 += v[i]; s1 += v[8 + i]; }
    const float m = wave_sum(lane < 16 ? s0 + s1 : s0) * (1.f / D);
    s0 = 0.f; s1 = 0.f;
#pragma unroll
    for (int i = 0; i < 8; ++i) { s0 += (v[i] - m) * (v[i] - m); s1 += (v[8 + i] - m) * (v[8 + i] - m); }
    const float var = wave_sum(lane < 16 ? s0 + s1 : s0) * (1.f / D);
    stat = pk(m, __builtin_sqrtf(rb(rb(var) + 1e-5f)));
  }
  WAITV(13);
  v4u sf[4];
#pragma unroll
  for (int t = 0; t < 4; ++t) sf[t] = trd2(L + 2 * BUF + w * 4096 + (lane >> 2) * 128 + (lane & 3) * 8 + t * 32, 16 * 128);
  if (w >= 3) {
    const v4u ones = {0x3f803f80u, 0x3f803f80u, 0x3f803f80u, 0x3f803f80u};
#pragma unroll
    for (int t = 0; t < 4; ++t) {
      f32x4 cs = mfma32(sf[t], ones, f32x4{0, 0, 0, 0});
      if (tok == 0) *(LAS f32x4*)(L + L_CP + (w - 3) * 256 + t * 64 + g * 16) = cs;
    }
  }
  WAITV(7);
  st8(c.rw, lane == 0 ? WS_RS + (panel * 128 + cg * 8 + w) * 8 : 0xfffffff0u, v2u{stat, c.tag});
  if (w < 3) {
#pragma unroll
    for (int t = 0; t < 4; ++t) st16(c.rw, pks0 < NKS && b < 250 ? WS_WT + ((4 * pcb + t) * NKS + pks) * 1024 + lane * 16 : 0xffffffe0u, sf[t]);
  }
  WAITL0(); BAR(); SB();

  v4u wpre[4][5];
  f32x4 pq[5];
  float mb[2], rs[2];
  const int dd = (cg & 1) * 32;
  if (!KV && w < 4) {
    f32x4 acc[2][8];
    gemm1<0, 8>(p, c, 0, 256, acc, wpre, mb, rs);
    const int j = cg - 8, ks0 = j < 7 ? 17 + 5 * j : 51;
#pragma unroll
    for (int u = 0; u < 4; ++u) hidden(c, acc[0][2 * u], acc[0][2 * u + 1], acc[1][2 * u], acc[1][2 * u + 1], 256 + 64 * u, ks0 + u, u < 3 || j < 6, mb, rs);
  } else {
    f32x4 acc[2][7];
    const int pbase = (w < 4 ? 192 : 0) + dd, nbase = KV ? (w < 4 ? 384 : 448) : 192;
    gemm1<5, 2>(p, c, pbase, nbase, acc, wpre, mb, rs);
#pragma unroll
    for (int t = 0; t < 5; ++t) {
      const f32x4 cs = *(LAS f32x4*)(L + L_CS + pbase * 2 + g * 80 + t * 16);
#pragma unroll
      for (int a = 0; a < 2; ++a) acc[a][t] = (acc[a][t] - mb[a] * cs) * rs[a];
    }
    v4u bq[2][3];
#pragma unroll
    for (int a = 0; a < 2; ++a) {
      bq[a][0] = pk8(acc[a][0], acc[a][1]); bq[a][1] = pk8(acc[a][2], acc[a][3]);
      const v2u e = pk4(acc[a][4]); bq[a][2] = v4u{e.x, e.y, 0x00003f80u, 0u};
    }
    const int ks = KV ? 2 * cg + (w >> 2) : (cg < 15 ? 5 * cg - 24 : 50);
    if (w < 4) {
#pragma unroll
      for (int a = 0; a < 2; ++a) {
        LAS char* d = L + L_V + (w * 32 + a * 16 + tok) * PV + g * 48;
        *(LAS v4u*)(d) = bq[a][0]; *(LAS v4u*)(d + 16) = bq[a][1]; *(LAS v4u*)(d + 32) = bq[a][2];
      }
      hidden(c, acc[0][5], acc[0][6], acc[1][5], acc[1][6], nbase, ks, true, mb, rs);
    } else {
      LAS char* wp = L + (((lane & 3) == g) ? L_WPHI + g * 800 + ((lane & 15) >> 2) * 40 : L_ZERO);
#pragma unroll
      for (int t = 0; t < 5; ++t) {
        const v4u a0 = trd2(wp + t * 8, 160), a1 = trd2(wp + 320 + t * 8, 160);
        const v2u a2 = trd(wp + 640 + t * 8);
        const v4u a2z = {a2.x, a2.y, 0u, 0u};
        f32x4 o = mfma32(a2z, bq[1][2], mfma32(a1, bq[1][1], mfma32(a0, bq[1][0], f32x4{0, 0, 0, 0})));
        *(LAS f32x4*)(L + L_P + (w - 4) * 5120 + t * 1024 + lane * 16) = o;
        pq[t] = mfma32(a2z, bq[0][2], mfma32(a1, bq[0][1], mfma32(a0, bq[0][0], f32x4{0, 0, 0, 0})));
      }
      hidden(c, acc[0][5], acc[0][6], acc[1][5], acc[1][6], nbase, ks, true, mb, rs);
    }
  }
  WAITL0(); BAR(); SB();
  if (w < 4) {
#pragma unroll
    for (int t = 0; t < 5; ++t) pq[t] = *(LAS f32x4*)(L + L_P + w * 5120 + t * 1024 + lane * 16);
  }
  {
    unsigned wv[20];
    dual_softmax(pq, wv);
    const int tt = (w & 3) * 2 + (w < 4);
    SB();
    WAITV(0);
    if (KV) {
      LAS char* d = L + L_PHK + (tt * 16 + tok) * PK + g * 80;
      *(LAS v4u*)(d) = v4u{wv[0], wv[1], wv[4], wv[5]};
      *(LAS v4u*)(d + 16) = v4u{wv[8], wv[9], wv[12], wv[13]};
      *(LAS v4u*)(d + 32) = v4u{wv[2], wv[3], wv[6], wv[7]};
      *(LAS v4u*)(d + 48) = v4u{wv[10], wv[11], wv[14], wv[15]};
      *(LAS v4u*)(d + 64) = v4u{wv[16], wv[17], wv[18], wv[19]};
    } else {
      const unsigned d = WS_PHQ + ((panel * 8 + tt) * 32 + (cg - 8) * 4 + g) * 1280 + tok * 16;
#pragma unroll
      for (int i = 0; i < 5; ++i) st16(c.rw, d + i * 256, v4u{wv[4 * i], wv[4 * i + 1], wv[4 * i + 2], wv[4 * i + 3]});
    }
  }
  WAITL0(); BAR(); SB();
  if (tid == 0) st4(c.rw, FL_H + pid * 64, c.tag);
  if (KV) {
    const int hd = w & 3, dt = w >> 2;
    f32x4 kv[3];
#pragma unroll
    for (int T = 0; T < 3; ++T) kv[T] = f32x4{0, 0, 0, 0};
    LAS char* A = L + L_PHK + (lane >> 2) * PK + hd * 80 + (lane & 3) * 8;
    LAS char* Bv = L + L_V + (lane >> 2) * PV + hd * 48 + dt * 32 + (lane & 3) * 8;
#pragma unroll
    for (int s = 0; s < 4; ++s) {
      const v4u b0 = trd2(Bv + s * 32 * PV, 16 * PV);
#pragma unroll
      for (int T = 0; T < 3; ++T) kv[T] = mfma32(trd2(A + s * 32 * PK + T * 32, 16 * PK), b0, kv[T]);
    }
    const unsigned d = WS_KVP + ((cg * 4 + hd) * 16 + panel) * KVB + (dt ? 2560 + (g * 5 + tok) * 16 : lane * 16);
    const bool on = !dt || tok < 5;
#pragma unroll
    for (int T = 0; T < 3; ++T) st16(c.rw, on && (T < 2 || lane < 32) ? d + T * (dt ? 320 : 1024) : 0xffffffe0u, __builtin_bit_cast(v4u, kv[T]));
  }
  WAITV(0); WAITL0(); BAR(); SB();
  if (tid == 0) st4(c.rw, FL_A + pid * 64, c.tag);

  const int rt = c.rt, ct = c.ct;
  const unsigned afr = WS_AFR + rt * 4 * NKS * 1024 + lane * 16, wt = WS_WT + ct * 5 * NKS * 1024 + lane * 16;
  f32x4 acc[4][5];
  auto zero = [&]() {
#pragma unroll
    for (int i = 0; i < 4; ++i)
#pragma unroll
      for (int j = 0; j < 5; ++j) acc[i][j] = f32x4{0, 0, 0, 0};
  };
  LAS char* myslot = L + (w & 3) * SLOT + tok * RP + g * 16;
  if (w < 4) {
    constexpr int NK = 13, DA = 3;
    wait_flags(c.rw, FL_H + ((rt >> 1) * 16 + tok) * 64, c.tag);
    const unsigned ap = afr + w * NK * 1024, bp = wt + w * NK * 1024;
    v4u ra[DA][4];
#pragma unroll
    for (int d = 0; d < DA; ++d)
#pragma unroll
      for (int i = 0; i < 4; ++i) ra[d][i] = ld16(c.rw, ap + (i * NKS + d) * 1024);
    zero();
    SB();
#pragma unroll
    for (int k = 0; k < NK; ++k) {
#pragma unroll
      for (int i = 0; i < 4; ++i)
#pragma unroll
        for (int j = 0; j < 5; ++j) acc[i][j] = mfma32(wpre[k % 4][j], ra[k % DA][i], acc[i][j]);
      SB();
      if (k + DA < NK) {
#pragma unroll
        for (int i = 0; i < 4; ++i) ra[k % DA][i] = ld16(c.rw, ap + (i * NKS + k + DA) * 1024);
      }
      if (k + 4 < NK) {
#pragma unroll
        for (int j = 0; j < 5; ++j) wpre[k % 4][j] = ld16(c.rw, bp + (j * NKS + k + 4) * 1024);
      }
      SB();
    }
#pragma unroll
    for (int i = 0; i < 4; ++i)
#pragma unroll
      for (int j = 0; j < 5; ++j) *(LAS f32x4*)(myslot + i * 16 * RP + j * 64) = acc[i][j];
    WAITL0(); BAR();
  } else {
    const int aw = w - 4;
    const int head = 4 * xcd + (r & 3), tb = r >> 2;
    LAS unsigned* cnt = (LAS unsigned*)(L + L_CNT);
    wait_flags(c.rw, FL_A + (lane < 4 ? (aw * 4 + lane) * 16 + (head >> 2) : (tb * 2 + (aw >> 1)) * 16 + 8 + (head >> 2)) * 64, c.tag);
    f32x4 kp[4][6];
    const unsigned kb = WS_KVP + (head * 16 + aw * 4) * KVB;
    const unsigned k0 = kb + lane * 16, k1 = tok < 5 ? kb + 2560 + (g * 5 + tok) * 16 : 0xf0000000u;
    const unsigned k0h = lane < 32 ? k0 : 0xf0000000u, k1h = lane < 32 ? k1 : 0xf0000000u;
#pragma unroll
    for (int q = 0; q < 4; ++q) {
      kp[q][0] = __builtin_bit_cast(f32x4, ld16(c.rw, k0 + q * KVB)); kp[q][2] = __builtin_bit_cast(f32x4, ld16(c.rw, k0 + q * KVB + 1024));
      kp[q][4] = __builtin_bit_cast(f32x4, ld16(c.rw, k0h + q * KVB + 2048));
      kp[q][1] = __builtin_bit_cast(f32x4, ld16(c.rw, k1 + q * KVB)); kp[q][3] = __builtin_bit_cast(f32x4, ld16(c.rw, k1 + q * KVB + 320));
      kp[q][5] = __builtin_bit_cast(f32x4, ld16(c.rw, k1h + q * KVB + 640));
    }
    v4u q0[4]; v2u q1[4];
#pragma unroll
    for (int i = 0; i < 4; ++i) {
      const unsigned qb = WS_PHQ + ((tb * 16 + aw * 4 + i) * 32 + head) * 1280;
      q0[i] = ld16(c.rw, qb + lane * 16); q1[i] = ld8(c.rw, qb + 1024 + tok * 16 + (g & 1) * 8);
    }
    SB();
#pragma unroll
    for (int t = 0; t < 6; ++t) *(LAS f32x4*)(L + L_KV + aw * 6144 + t * 1024 + lane * 16) = (kp[0][t] + kp[1][t]) + (kp[2][t] + kp[3][t]);
    WAITL0();
    if (lane == 0) __hip_atomic_fetch_add(cnt, 1u, __ATOMIC_RELAXED, __HIP_MEMORY_SCOPE_WORKGROUP);
    while (__hip_atomic_load(cnt, __ATOMIC_RELAXED, __HIP_MEMORY_SCOPE_WORKGROUP) < 4u) __builtin_amdgcn_s_sleep(1);
    asm volatile("" ::: "memory");
    f32x4 kv[6];
#pragma unroll
    for (int t = 0; t < 6; ++t) {
      LAS char* s = L + L_KV + t * 1024 + lane * 16;
      kv[t] = (*(LAS f32x4*)(s) + *(LAS f32x4*)(s + 6144)) + (*(LAS f32x4*)(s + 2 * 6144) + *(LAS f32x4*)(s + 3 * 6144));
    }
    v4u A0[2]; v2u A1[2];
#pragma unroll
    for (int dt = 0; dt < 2; ++dt) { A0[dt] = pk8(kv[dt], kv[2 + dt]); A1[dt] = pk4(kv[4 + dt]); }
    const int Qa = 5 * head + g, Qb = 5 * head + 4;
    const unsigned oa = WS_AFR + (54 + (Qa >> 3)) * 1024 + (((Qa & 7) >> 1) * 16 + tok) * 16 + (Qa & 1) * 8;
    const unsigned ob = g == 0 ? WS_AFR + (54 + (Qb >> 3)) * 1024 + (((Qb & 7) >> 1) * 16 + tok) * 16 + (Qb & 1) * 8 : 0xf0000000u;
#pragma unroll
    for (int i = 0; i < 4; ++i) {
      const f32x4 o0 = mfma16(A1[0], q1[i], mfma32(A0[0], q0[i], f32x4{0, 0, 0, 0}));
      const f32x4 o1 = mfma16(A1[1], q1[i], mfma32(A0[1], q0[i], f32x4{0, 0, 0, 0}));
      const float den = __builtin_bit_cast(float, __builtin_amdgcn_ds_bpermute((16 + tok) * 4, __builtin_bit_cast(int, o1[0])));
      const float rd = 1.f / (den + 1e-6f);
      const unsigned tbase = (tb * 16 + aw * 4 + i) * NKS * 1024;
      st8(c.rw, tbase + oa, pk4(o0 * rd));
      st8(c.rw, tbase + ob, pk4(o1 * rd));
    }
    WAITV(0);
    if (lane == 0) {
      const unsigned old = __hip_atomic_fetch_add(cnt + 1, 1u, __ATOMIC_RELAXED, __HIP_MEMORY_SCOPE_WORKGROUP);
      if (old == 3u) st4(c.rw, FL_O + (tb * 32 + head) * 64, c.tag);
    }
    zero();
    v4u w4[5];
    if (aw < 2) {
      wait_flags(c.rw, FL_H + ((rt >> 1) * 16 + 15) * 64, c.tag);
      v4u ea[4];
#pragma unroll
      for (int i = 0; i < 4; ++i) ea[i] = ld16(c.rw, afr + (i * NKS + 52 + aw) * 1024);
#pragma unroll
      for (int j = 0; j < 5; ++j) w4[j] = ld16(c.rw, wt + (j * NKS + 52 + aw) * 1024);
#pragma unroll
      for (int i = 0; i < 4; ++i)
#pragma unroll
        for (int j = 0; j < 5; ++j) acc[i][j] = mfma32(w4[j], ea[i], acc[i][j]);
    }
    SB();
#pragma unroll
    for (int j = 0; j < 5; ++j) w4[j] = ld16(c.rw, wt + (j * NKS + 58 + aw * 5) * 1024);
    wait_flags(c.rw, FL_O + ((rt >> 2) * 32 + aw * 8 + (lane & 7)) * 64, c.tag);
    v4u ra[3][4];
    const unsigned abase = afr + (54 + aw * 5) * 1024;
#pragma unroll
    for (int k = 0; k < 3; ++k)
#pragma unroll
      for (int i = 0; i < 4; ++i) ra[k][i] = ld16(c.rw, abase + (i * NKS + k) * 1024);
    SB();
#pragma unroll
    for (int k = 0; k < 5; ++k) {
#pragma unroll
      for (int i = 0; i < 4; ++i)
#pragma unroll
        for (int j = 0; j < 5; ++j) acc[i][j] = mfma32(k < 4 ? wpre[k][j] : w4[j], ra[k % 3][i], acc[i][j]);
      SB();
      if (k < 2) {
#pragma unroll
        for (int i = 0; i < 4; ++i) ra[k][i] = ld16(c.rw, abase + (i * NKS + k + 3) * 1024);
      }
      SB();
    }
    BAR();
#pragma unroll
    for (int i = 0; i < 4; ++i)
#pragma unroll
      for (int j = 0; j < 5; ++j) acc[i][j] += *(LAS f32x4*)(myslot + i * 16 * RP + j * 64);
#pragma unroll
    for (int i = 0; i < 4; ++i)
#pragma unroll
      for (int j = 0; j < 5; ++j) *(LAS f32x4*)(myslot + i * 16 * RP + j * 64) = acc[i][j];
  }
  WAITL0(); BAR();
  for (int task = tid; task < 640; task += 512) {
    const int row = task / 10, cc = task - row * 10;
    LAS char* s = L + row * RP + cc * 32;
    const f32x4 a = (*(LAS f32x4*)(s) + *(LAS f32x4*)(s + SLOT)) + (*(LAS f32x4*)(s + 2 * SLOT) + *(LAS f32x4*)(s + 3 * SLOT));
    const f32x4 bq = (*(LAS f32x4*)(s + 16) + *(LAS f32x4*)(s + SLOT + 16)) + (*(LAS f32x4*)(s + 2 * SLOT + 16) + *(LAS f32x4*)(s + 3 * SLOT + 16));
    const size_t go = (size_t)(rt * 64 + row) * (D * 2) + ct * 160 + cc * 16;
    const v4u xv = *(const v4u*)((const char*)p.x + go), bv = *(const v4u*)((const char*)p.b2 + ct * 160 + cc * 16);
    v4u o;
    o.x = pk(a[0] + lo(xv.x) + lo(bv.x), a[1] + hi(xv.x) + hi(bv.x)); o.y = pk(a[2] + lo(xv.y) + lo(bv.y), a[3] + hi(xv.y) + hi(bv.y));
    o.z = pk(bq[0] + lo(xv.z) + lo(bv.z), bq[1] + hi(xv.z) + hi(bv.z)); o.w = pk(bq[2] + lo(xv.w) + lo(bv.w), bq[3] + hi(xv.w) + hi(bv.w));
    *(v4u*)((char*)p.out + go) = o;
  }
  if (b == 0 && w == 7) {
#pragma unroll
    for (int i = 0; i < 4; ++i) wait_flags(c.rw, FL_H + (i * 64 + lane) * 64, c.tag);
    if (lane == 0) st4(c.rw, WS_EPOCH, c.tag);
  }
}
}

extern "C" void kernel_launch(void* const* d_in, const int* in_sizes, int n_in, void* d_out, int out_size, void* d_ws, size_t ws_size, hipStream_t stream) {
  using namespace hk;
  P p;
  p.x = (const u16*)d_in[0]; p.wqkv = (const u16*)d_in[1]; p.wphi = (const u16*)d_in[2]; p.wo = (const u16*)d_in[3];
  p.w1 = (const u16*)d_in[5]; p.b1 = (const u16*)d_in[6]; p.w2 = (const u16*)d_in[7]; p.b2 = (const u16*)d_in[8];
  p.out = (u16*)d_out; p.ws = (char*)d_ws;
  (void)hipFuncSetAttribute((const void*)fk, hipFuncAttributeMaxDynamicSharedMemorySize, L_BYTES);
  fk<<<256, 512, L_BYTES, stream>>>(p);
}
```

```cpp
#define NOROT
#include <hip/hip_runtime.h>
#include <hip/hip_bf16.h>
#include <stdint.h>

#define GAS __attribute__((address_space(1)))
#define LAS __attribute__((address_space(3)))
#define DI __device__ __forceinline__
typedef unsigned short u16;
typedef unsigned long long u64;
typedef unsigned v4u __attribute__((ext_vector_type(4)));
typedef unsigned v2u __attribute__((ext_vector_type(2)));
typedef float f32x4 __attribute__((ext_vector_type(4)));
typedef unsigned v8u __attribute__((ext_vector_type(8)));
typedef unsigned v16u __attribute__((ext_vector_type(16)));
typedef float f32x8 __attribute__((ext_vector_type(8)));
typedef float f32x16 __attribute__((ext_vector_type(16)));
typedef float f32x32 __attribute__((ext_vector_type(32)));
typedef float f32x2 __attribute__((ext_vector_type(2)));
typedef short s16x4 __attribute__((ext_vector_type(4)));
typedef short s16x8 __attribute__((ext_vector_type(8)));
typedef __bf16 bf16x2 __attribute__((ext_vector_type(2)));

namespace hk {
constexpr int S = 2048, D = 640, F = 1728, NKS = 74;
constexpr int NCOL = 3 * D + F;
constexpr unsigned WS_EPOCH = 0;
constexpr unsigned WS_FLAG = 4096;
constexpr unsigned FL_PT = WS_FLAG;
constexpr unsigned MB_H = WS_FLAG + 4096, MB_O = MB_H + 128;
constexpr unsigned MB_K = MB_H + 65536, MB_Q = MB_K + 64;
constexpr unsigned WS_RS = MB_K + 32768;
constexpr unsigned WS_CS = WS_RS + S * 8;
constexpr unsigned WS_AFR = 262144;
constexpr unsigned WS_WT = WS_AFR + 128 * NKS * 1024;
constexpr unsigned WS_PHQ = WS_WT + 40 * NKS * 1024;
constexpr unsigned KVB = 3456;
constexpr unsigned WS_KVP = WS_PHQ + 128 * 32 * 1280;
constexpr unsigned WS_END = WS_KVP + 32 * 16 * KVB;
static_assert(WS_CS + 4 * NCOL * 8 <= WS_AFR, "ws");
constexpr int XB = 16384, WR = 1056, BUF = XB + 32 * WR;
constexpr int L_WPHI = 3 * BUF, L_ZERO = L_WPHI + 3328, L_CS = L_ZERO + 1024, L_B1 = L_CS + 1024, L_CP = L_B1 + 1024, L_CNT = L_CP + 1280, L_KV = L_CNT + 64, L_BYTES = L_KV + 3072 + 1024;
constexpr int PK = 352, PV = 224;
constexpr int L_V = 3 * BUF - 128 * PV - 64, L_PHK = L_V - 128 * PK - 64;
constexpr int NST = 18, L_XB = L_PHK;
constexpr int C_H = L_CNT, C_K = L_CNT + 4, C_V = L_CNT + 8, C_Q = L_CNT + 12, C_A = L_CNT + 16, C_B = L_CNT + 20, C_X = L_CNT + 24;
constexpr unsigned OOB = 0xf0000000u;
static_assert(L_PHK >= BUF && 4 * NST * 1024 <= L_PHK && L_XB + 61440 <= 3 * BUF && L_BYTES <= 163840, "lds");

DI f32x4 mfma32(v4u a, v4u b, f32x4 c) { return __builtin_amdgcn_mfma_f32_16x16x32_bf16(__builtin_bit_cast(s16x8, a), __builtin_bit_cast(s16x8, b), c, 0, 0, 0); }
DI f32x4 mfma16(v2u a, v2u b, f32x4 c) { return mfma32(v4u{a.x, a.y, 0u, 0u}, v4u{b.x, b.y, 0u, 0u}, c); }
DI v2u trd(LAS char* p) { return __builtin_bit_cast(v2u, __builtin_amdgcn_ds_read_tr16_b64_v4i16((LAS s16x4*)p)); }
DI v4u trd2(LAS char* p, int d) { v2u a = trd(p), b = trd(p + d); return v4u{a.x, a.y, b.x, b.y}; }
DI unsigned pk(float a, float b) { f32x2 v = {a, b}; return __builtin_bit_cast(unsigned, __builtin_convertvector(v, bf16x2)); }
DI v2u pk4(f32x4 a) { return v2u{pk(a[0], a[1]), pk(a[2], a[3])}; }
DI v4u pk8(f32x4 a, f32x4 b) { return v4u{pk(a[0], a[1]), pk(a[2], a[3]), pk(b[0], b[1]), pk(b[2], b[3])}; }
DI float lo(unsigned u) { return __builtin_bit_cast(float, u << 16); }
DI float hi(unsigned u) { return __builtin_bit_cast(float, u & 0xffff0000u); }
DI float rb(float a) { return lo(pk(a, a)); }
DI void glds(const void* g, unsigned dst) { asm volatile("s_mov_b32 m0, %1\n\ts_nop 0\n\tglobal_load_lds_dwordx4 %0, off" ::"v"(g), "s"(dst) : "memory"); }
DI void gldso(const void* base, unsigned off, unsigned dst) { asm volatile("s_mov_b32 m0, %2\n\ts_nop 0\n\tglobal_load_lds_dwordx4 %0, %1" ::"v"(off), "s"(base), "s"(dst) : "memory"); }
#define BAR() __builtin_amdgcn_s_barrier()
#define WAITV(n) asm volatile("s_waitcnt vmcnt(" #n ")" ::: "memory")
#define WAITL0() asm volatile("s_waitcnt lgkmcnt(0)" ::: "memory")
#define SB() __builtin_amdgcn_sched_barrier(0)
typedef __amdgpu_buffer_rsrc_t rsrc_t;
DI v4u ld16(rsrc_t r, unsigned off) { return __builtin_amdgcn_raw_buffer_load_b128(r, off, 0, 16); }
DI v4u ld16s(rsrc_t r, unsigned off, unsigned so) { return __builtin_amdgcn_raw_buffer_load_b128(r, off, so, 16); }
DI v2u ld8(rsrc_t r, unsigned off) { return __builtin_amdgcn_raw_buffer_load_b64(r, off, 0, 16); }
DI unsigned ld4(rsrc_t r, unsigned off) { return __builtin_amdgcn_raw_buffer_load_b32(r, off, 0, 16); }
DI void st16(rsrc_t r, unsigned off, v4u v) { __builtin_amdgcn_raw_buffer_store_b128(v, r, off, 0, 16); }
DI void st8(rsrc_t r, unsigned off, v2u v) { __builtin_amdgcn_raw_buffer_store_b64(v, r, off, 0, 16); }
DI void st4(rsrc_t r, unsigned off, unsigned v) { __builtin_amdgcn_raw_buffer_store_b32(v, r, off, 0, 16); }
constexpr unsigned SPINS = 1u << 16;
#define INF 8
#define RA 8
DI void wait_tail(rsrc_t r, unsigned off, unsigned tag) {
#pragma unroll 1
  for (unsigned i = 0; i < SPINS; ++i) {
    asm volatile("" ::: "memory");
    if (__builtin_amdgcn_ballot_w64(ld4(r, off) != tag) == 0) break;
  }
}
template <int N, class V> DI unsigned differs(const V& v, unsigned tag) {
  u64 a = ~0ull, o = 0;
#pragma unroll
  for (int i = 0; i < N; i += 2) { const u64 x = (u64)v[i + 1] << 32 | v[i]; a &= x; o |= x; }
  unsigned l = (unsigned)a & (unsigned)(a >> 32), h = (unsigned)o | (unsigned)(o >> 32);
  asm("" : "+s"(l), "+s"(h));
  return (l ^ tag) | (h ^ tag);
}
template <int N> DI void wait_flags(const char* ws, rsrc_t r, unsigned off, unsigned off1, unsigned tag) {
  unsigned d = 1;
#pragma unroll 1
  for (unsigned i = 0; i < 4096 && d; ++i) {
    if (N == 8) { v8u v; asm volatile("s_load_dwordx8 %0, %1, %2 glc\n\ts_waitcnt lgkmcnt(0)" : "=s"(v) : "s"(ws), "s"(off) : "memory"); d = differs<8>(v, tag); }
    else if (N == 16) { v16u v; asm volatile("s_load_dwordx16 %0, %1, %2 glc\n\ts_waitcnt lgkmcnt(0)" : "=s"(v) : "s"(ws), "s"(off) : "memory"); d = differs<16>(v, tag); }
    else { v16u v; unsigned u; asm volatile("s_load_dwordx16 %0, %2, %3 glc\n\ts_load_dword %1, %2, %4 glc\n\ts_waitcnt lgkmcnt(0)" : "=&s"(v), "=&s"(u) : "s"(ws), "s"(off), "s"(off1) : "memory"); d = differs<16>(v, tag) | (u ^ tag); }
  }
  if (d) { const unsigned l = threadIdx.x & 63; wait_tail(r, N == 17 && l == 16 ? off1 : off + (l & (N == 8 ? 7 : 15)) * 4, tag); }
}
DI void arrive(int c) { asm volatile("s_mov_b64 exec, 1\n\tds_add_u32 %0, %1\n\ts_mov_b64 exec, -1" ::"v"(c), "v"(1u) : "memory"); }
DI void arrive_flag(int c, unsigned last, rsrc_t r, unsigned off, unsigned tag) {
  unsigned t, u;
  asm volatile("s_mov_b64 exec, 1\n\tds_add_rtn_u32 %0, %2, %3\n\ts_mov_b64 exec, -1\n\ts_waitcnt lgkmcnt(0)\n\tv_readfirstlane_b32 %1, %0\n\ts_cmp_lg_u32 %1, %4\n\ts_cbranch_scc1 1f\n\tbuffer_store_dword %5, %6, %7, 0 offen sc1\n1:"
               : "=&v"(t), "=&s"(u) : "v"(c), "v"(1u), "n"(last), "v"(tag), "v"(off), "s"(r) : "memory", "scc");
}
DI unsigned peekv(LAS char* L, int c) { return __hip_atomic_load((LAS unsigned*)(L + c), __ATOMIC_RELAXED, __HIP_MEMORY_SCOPE_WORKGROUP); }
DI unsigned peek(LAS char* L, int c) { return __builtin_amdgcn_readfirstlane(peekv(L, c)); }
DI void await(LAS char* L, int c, unsigned n) {
  #pragma unroll 1
  for (unsigned i = 0; i < (1u << 22) && peek(L, c) < n; ++i) __builtin_amdgcn_s_sleep(1);
  asm volatile("" ::: "memory");
}
DI unsigned sel(bool c, unsigned a, unsigned b) { return c ? a : b; }
DI v4u sub4(const v16u& v, int i) { return v4u{v[4 * i], v[4 * i + 1], v[4 * i + 2], v[4 * i + 3]}; }
#define O_ISSUE0() asm volatile( \
        "buffer_load_dwordx4 v[176:179], %[fo], %[rw], %[so] offen sc1\n\t" "buffer_load_dwordx4 v[180:183], %[fo], %[rw], %[so] offen offset:1024 sc1\n\t"  \
        "buffer_load_dwordx4 v[184:187], %[fo], %[rw], %[so] offen offset:2048 sc1\n\t" "buffer_load_dwordx4 v[188:191], %[fo], %[rw], %[so] offen offset:3072 sc1\n\t"  \
        "buffer_load_dwordx2 v[192:193], %[ro], %[rw], %[so] offen sc1\n\t" "buffer_load_dwordx2 v[194:195], %[ro], %[rw], %[so] offen offset:128 sc1\n\t" "" \
        : "=&{v[176:191]}"(of[0]), "=&{v[192:195]}"(og[0]) : [fo] "v"(fo), [ro] "v"(ro), [rw] "s"(c.rw), [so] "s"(so[0]) : "memory")
#define O_GET0() asm volatile( \
        "s_mov_b32 %[it], %[sp]\n\t" "s_waitcnt vmcnt(18)\n\t" "1:\tv_max3_u32 %[t0], v176, v177, v178\n\t" "v_max3_u32 %[t1], v179, v180, v181\n\t" "v_max3_u32 %[t0], %[t0], v182, v183\n\t"  \
        "v_max3_u32 %[t1], %[t1], v184, v185\n\t" "v_max3_u32 %[t0], %[t0], v186, v187\n\t" "v_max3_u32 %[t1], %[t1], v188, v189\n\t" "v_max3_u32 %[t0], %[t0], v190, v191\n\t"  \
        "v_max3_u32 %[t1], %[t1], v192, v193\n\t" "v_max3_u32 %[t0], %[t0], v194, v195\n\t" "v_max_u32 %[t0], %[t0], %[t1]\n\t" "v_cmp_eq_u32 vcc, -1, %[t0]\n\t" "s_cbranch_vccz 2f\n\t"  \
        "buffer_load_dwordx4 v[176:179], %[fo], %[rw], %[so] offen sc1\n\t" "buffer_load_dwordx4 v[180:183], %[fo], %[rw], %[so] offen offset:1024 sc1\n\t"  \
        "buffer_load_dwordx4 v[184:187], %[fo], %[rw], %[so] offen offset:2048 sc1\n\t" "buffer_load_dwordx4 v[188:191], %[fo], %[rw], %[so] offen offset:3072 sc1\n\t"  \
        "buffer_load_dwordx2 v[192:193], %[ro], %[rw], %[so] offen sc1\n\t" "buffer_load_dwordx2 v[194:195], %[ro], %[rw], %[so] offen offset:128 sc1\n\t" "s_waitcnt vmcnt(0)\n\t"  \
        "s_sub_u32 %[it], %[it], 1\n\t" "s_cbranch_scc0 1b\n\t" "2:" \
        : "+{v[176:191]}"(of[0]), "+{v[192:195]}"(og[0]), [t0] "=&v"(t0), [t1] "=&v"(t1), [it] "=&s"(it) \
        : [fo] "v"(fo), [ro] "v"(ro), [rw] "s"(c.rw), [so] "s"(so[0]), [sp] "n"(SPINS) : "memory", "vcc", "scc")
#define O_ISSUE1() asm volatile( \
        "buffer_load_dwordx4 v[196:199], %[fo], %[rw], %[so] offen sc1\n\t" "buffer_load_dwordx4 v[200:203], %[fo], %[rw], %[so] offen offset:1024 sc1\n\t"  \
        "buffer_load_dwordx4 v[204:207], %[fo], %[rw], %[so] offen offset:2048 sc1\n\t" "buffer_load_dwordx4 v[208:211], %[fo], %[rw], %[so] offen offset:3072 sc1\n\t"  \
        "buffer_load_dwordx2 v[212:213], %[ro], %[rw], %[so] offen sc1\n\t" "buffer_load_dwordx2 v[214:215], %[ro], %[rw], %[so] offen offset:128 sc1\n\t" "" \
        : "=&{v[196:211]}"(of[1]), "=&{v[212:215]}"(og[1]) : [fo] "v"(fo), [ro] "v"(ro), [rw] "s"(c.rw), [so] "s"(so[1]) : "memory")
#define O_GET1() asm volatile( \
        "s_mov_b32 %[it], %[sp]\n\t" "s_waitcnt vmcnt(12)\n\t" "1:\tv_max3_u32 %[t0], v196, v197, v198\n\t" "v_max3_u32 %[t1], v199, v200, v201\n\t" "v_max3_u32 %[t0], %[t0], v202, v203\n\t"  \
        "v_max3_u32 %[t1], %[t1], v204, v205\n\t" "v_max3_u32 %[t0], %[t0], v206, v207\n\t" "v_max3_u32 %[t1], %[t1], v208, v209\n\t" "v_max3_u32 %[t0], %[t0], v210, v211\n\t"  \
        "v_max3_u32 %[t1], %[t1], v212, v213\n\t" "v_max3_u32 %[t0], %[t0], v214, v215\n\t" "v_max_u32 %[t0], %[t0], %[t1]\n\t" "v_cmp_eq_u32 vcc, -1, %[t0]\n\t" "s_cbranch_vccz 2f\n\t"  \
        "buffer_load_dwordx4 v[196:199], %[fo], %[rw], %[so] offen sc1\n\t" "buffer_load_dwordx4 v[200:203], %[fo], %[rw], %[so] offen offset:1024 sc1\n\t"  \
        "buffer_load_dwordx4 v[204:207], %[fo], %[rw], %[so] offen offset:2048 sc1\n\t" "buffer_load_dwordx4 v[208:211], %[fo], %[rw], %[so] offen offset:3072 sc1\n\t"  \
        "buffer_load_dwordx2 v[212:213], %[ro], %[rw], %[so] offen sc1\n\t" "buffer_load_dwordx2 v[214:215], %[ro], %[rw], %[so] offen offset:128 sc1\n\t" "s_waitcnt vmcnt(0)\n\t"  \
        "s_sub_u32 %[it], %[it], 1\n\t" "s_cbranch_scc0 1b\n\t" "2:" \
        : "+{v[196:211]}"(of[1]), "+{v[212:215]}"(og[1]), [t0] "=&v"(t0), [t1] "=&v"(t1), [it] "=&s"(it) \
        : [fo] "v"(fo), [ro] "v"(ro), [rw] "s"(c.rw), [so] "s"(so[1]), [sp] "n"(SPINS) : "memory", "vcc", "scc")
#define O_ISSUE2() asm volatile( \
        "buffer_load_dwordx4 v[216:219], %[fo], %[rw], %[so] offen sc1\n\t" "buffer_load_dwordx4 v[220:223], %[fo], %[rw], %[so] offen offset:1024 sc1\n\t"  \
        "buffer_load_dwordx4 v[224:227], %[fo], %[rw], %[so] offen offset:2048 sc1\n\t" "buffer_load_dwordx4 v[228:231], %[fo], %[rw], %[so] offen offset:3072 sc1\n\t"  \
        "buffer_load_dwordx2 v[232:233], %[ro], %[rw], %[so] offen sc1\n\t" "buffer_load_dwordx2 v[234:235], %[ro], %[rw], %[so] offen offset:128 sc1\n\t" "" \
        : "=&{v[216:231]}"(of[2]), "=&{v[232:235]}"(og[2]) : [fo] "v"(fo), [ro] "v"(ro), [rw] "s"(c.rw), [so] "s"(so[2]) : "memory")
#define O_GET2() asm volatile( \
        "s_mov_b32 %[it], %[sp]\n\t" "s_waitcnt vmcnt(6)\n\t" "1:\tv_max3_u32 %[t0], v216, v217, v218\n\t" "v_max3_u32 %[t1], v219, v220, v221\n\t" "v_max3_u32 %[t0], %[t0], v222, v223\n\t"  \
        "v_max3_u32 %[t1], %[t1], v224, v225\n\t" "v_max3_u32 %[t0], %[t0], v226, v227\n\t" "v_max3_u32 %[t1], %[t1], v228, v229\n\t" "v_max3_u32 %[t0], %[t0], v230, v231\n\t"  \
        "v_max3_u32 %[t1], %[t1], v232, v233\n\t" "v_max3_u32 %[t0], %[t0], v234, v235\n\t" "v_max_u32 %[t0], %[t0], %[t1]\n\t" "v_cmp_eq_u32 vcc, -1, %[t0]\n\t" "s_cbranch_vccz 2f\n\t"  \
        "buffer_load_dwordx4 v[216:219], %[fo], %[rw], %[so] offen sc1\n\t" "buffer_load_dwordx4 v[220:223], %[fo], %[rw], %[so] offen offset:1024 sc1\n\t"  \
        "buffer_load_dwordx4 v[224:227], %[fo], %[rw], %[so] offen offset:2048 sc1\n\t" "buffer_load_dwordx4 v[228:231], %[fo], %[rw], %[so] offen offset:3072 sc1\n\t"  \
        "buffer_load_dwordx2 v[232:233], %[ro], %[rw], %[so] offen sc1\n\t" "buffer_load_dwordx2 v[234:235], %[ro], %[rw], %[so] offen offset:128 sc1\n\t" "s_waitcnt vmcnt(0)\n\t"  \
        "s_sub_u32 %[it], %[it], 1\n\t" "s_cbranch_scc0 1b\n\t" "2:" \
        : "+{v[216:231]}"(of[2]), "+{v[232:235]}"(og[2]), [t0] "=&v"(t0), [t1] "=&v"(t1), [it] "=&s"(it) \
        : [fo] "v"(fo), [ro] "v"(ro), [rw] "s"(c.rw), [so] "s"(so[2]), [sp] "n"(SPINS) : "memory", "vcc", "scc")
#define O_ISSUE3() asm volatile( \
        "buffer_load_dwordx4 v[236:239], %[fo], %[rw], %[so] offen sc1\n\t" "buffer_load_dwordx4 v[240:243], %[fo], %[rw], %[so] offen offset:1024 sc1\n\t"  \
        "buffer_load_dwordx4 v[244:247], %[fo], %[rw], %[so] offen offset:2048 sc1\n\t" "buffer_load_dwordx4 v[248:251], %[fo], %[rw], %[so] offen offset:3072 sc1\n\t"  \
        "buffer_load_dwordx2 v[252:253], %[ro], %[rw], %[so] offen sc1\n\t" "buffer_load_dwordx2 v[254:255], %[ro], %[rw], %[so] offen offset:128 sc1\n\t" "" \
        : "=&{v[236:251]}"(of[3]), "=&{v[252:255]}"(og[3]) : [fo] "v"(fo), [ro] "v"(ro), [rw] "s"(c.rw), [so] "s"(so[3]) : "memory")
#define O_GET3() asm volatile( \
        "s_mov_b32 %[it], %[sp]\n\t" "s_waitcnt vmcnt(0)\n\t" "1:\tv_max3_u32 %[t0], v236, v237, v238\n\t" "v_max3_u32 %[t1], v239, v240, v241\n\t" "v_max3_u32 %[t0], %[t0], v242, v243\n\t"  \
        "v_max3_u32 %[t1], %[t1], v244, v245\n\t" "v_max3_u32 %[t0], %[t0], v246, v247\n\t" "v_max3_u32 %[t1], %[t1], v248, v249\n\t" "v_max3_u32 %[t0], %[t0], v250, v251\n\t"  \
        "v_max3_u32 %[t1], %[t1], v252, v253\n\t" "v_max3_u32 %[t0], %[t0], v254, v255\n\t" "v_max_u32 %[t0], %[t0], %[t1]\n\t" "v_cmp_eq_u32 vcc, -1, %[t0]\n\t" "s_cbranch_vccz 2f\n\t"  \
        "buffer_load_dwordx4 v[236:239], %[fo], %[rw], %[so] offen sc1\n\t" "buffer_load_dwordx4 v[240:243], %[fo], %[rw], %[so] offen offset:1024 sc1\n\t"  \
        "buffer_load_dwordx4 v[244:247], %[fo], %[rw], %[so] offen offset:2048 sc1\n\t" "buffer_load_dwordx4 v[248:251], %[fo], %[rw], %[so] offen offset:3072 sc1\n\t"  \
        "buffer_load_dwordx2 v[252:253], %[ro], %[rw], %[so] offen sc1\n\t" "buffer_load_dwordx2 v[254:255], %[ro], %[rw], %[so] offen offset:128 sc1\n\t" "s_waitcnt vmcnt(0)\n\t"  \
        "s_sub_u32 %[it], %[it], 1\n\t" "s_cbranch_scc0 1b\n\t" "2:" \
        : "+{v[236:251]}"(of[3]), "+{v[252:255]}"(og[3]), [t0] "=&v"(t0), [t1] "=&v"(t1), [it] "=&s"(it) \
        : [fo] "v"(fo), [ro] "v"(ro), [rw] "s"(c.rw), [so] "s"(so[3]), [sp] "n"(SPINS) : "memory", "vcc", "scc")
template <int CTRL, int RM> DI float dpp_add(float v) { return v + __builtin_bit_cast(float, __builtin_amdgcn_update_dpp(0, __builtin_bit_cast(int, v), CTRL, RM, 0xf, false)); }
DI float wave_sum(float v) {
  v = dpp_add<0x111, 0xf>(v); v = dpp_add<0x112, 0xf>(v); v = dpp_add<0x114, 0xf>(v); v = dpp_add<0x118, 0xf>(v);
  v = dpp_add<0x142, 0xa>(v); v = dpp_add<0x143, 0xc>(v);
  return __builtin_bit_cast(float, __builtin_amdgcn_readlane(__builtin_bit_cast(int, v), 63));
}

struct P {
  const u16 *x, *wqkv, *wphi, *wo, *w1, *b1, *w2, *b2;
  u16* out;
  char* ws;
};

template <class F>
DI void dual_softmax(const f32x4 (&a)[5], float sc, unsigned (&w)[20], F&& mid) {
  float m0 = __builtin_fmaxf(a[0][0], a[0][1]), m1 = __builtin_fmaxf(a[0][2], a[0][3]), n0 = __builtin_fminf(a[0][0], a[0][1]), n1 = __builtin_fminf(a[0][2], a[0][3]);
#pragma unroll
  for (int i = 1; i < 5; ++i) {
    m0 = __builtin_fmaxf(__builtin_fmaxf(m0, a[i][0]), a[i][1]); m1 = __builtin_fmaxf(__builtin_fmaxf(m1, a[i][2]), a[i][3]);
    n0 = __builtin_fminf(__builtin_fminf(n0, a[i][0]), a[i][1]); n1 = __builtin_fminf(__builtin_fminf(n1, a[i][2]), a[i][3]);
  }
  const float L2E = 1.4426950408889634f * sc;
#pragma unroll
  for (int h = 0; h < 2; ++h) {
    const float c = h ? __builtin_fminf(n0, n1) * L2E : __builtin_fmaxf(m0, m1) * -L2E, k = h ? -L2E : L2E;
    f32x2 e[10];
#pragma unroll
    for (int i = 0; i < 10; ++i) e[i] = __builtin_elementwise_fma(f32x2{a[i >> 1][(i & 1) * 2], a[i >> 1][(i & 1) * 2 + 1]}, f32x2{k, k}, f32x2{c, c});
    SB();
#pragma unroll
    for (int i = 0; i < 10; ++i) e[i] = f32x2{__builtin_amdgcn_exp2f(e[i].x), __builtin_amdgcn_exp2f(e[i].y)};
    SB();
    const f32x2 s = (((e[0] + e[1]) + (e[2] + e[3])) + ((e[4] + e[5]) + (e[6] + e[7]))) + (e[8] + e[9]);
    const float r = __builtin_amdgcn_rcpf(s.x + s.y);
#pragma unroll
    for (int i = 0; i < 10; ++i) { const f32x2 v = e[i] * f32x2{r, r}; w[10 * h + i] = pk(v.x, v.y); }
    if (h == 0) { SB(); mid(); SB(); }
  }
}

struct Ctx {
  LAS char* L; rsrc_t rw; unsigned tag;
  int b, panel, cg, w, lane, rt, ct, head, tb, rot;
  const char* wp[4]; u64 wadv; const char* xb; unsigned xo0, xo1;
};
DI void piece(Ctx& c, int i, int dx, int dw) {
  if (i < 2) {
    asm volatile("s_add_i32 m0, %2, %3\n\ts_nop 0\n\tglobal_load_lds_dwordx4 %0, %1" ::"v"(i ? c.xo1 : c.xo0), "s"(c.xb), "s"(dx), "n"(i * 1024) : "memory", "scc");
    if (i) c.xb += 128;
  } else {
    asm volatile("s_add_i32 m0, %2, %3\n\tv_lshl_add_u64 %0, %0, 0, %1\n\tglobal_load_lds_dwordx4 %0, off" : "+v"(c.wp[i - 2]) : "v"(c.wadv), "s"(dw), "n"((i - 2) * WR) : "memory", "scc");
  }
}
DI int img_col(int cg, int ch) {
  const int win = ((160 * (cg & 7)) & ~63) >> 1, j = cg - 8;
  const int b0 = cg < 8 ? D + win : win;
  const int b1 = cg < 8 ? 2 * D + win - 96 : 3 * D + (j < 7 ? 512 + 160 * j : 1600) - 96;
  const int b2 = cg < 8 ? 3 * D + 64 * cg - 192 : b1;
  const int cc = j >= 6 && ch > 27 ? 27 : ch;
  return (ch < 12 ? b0 : ch < 24 ? b1 : b2) + 8 * cc;
}

template <int NP, int NN, bool ISB, class TF>
DI void gemm1(const P& p, Ctx& c, int pbase, int nbase, f32x4 (&acc)[2][NP + NN], v4u (&wpre)[6][5], float (&mb)[2], float (&rs)[2], TF&& tail) {
  constexpr int NT = NP + NN;
  LAS char* L = c.L;
  const int lane = c.lane, w = c.w, g = lane >> 4, tok = lane & 15;
  const int offN = XB + (lane >> 2) * WR + 8 * (lane & 3) + nbase, offP = XB + (lane >> 2) * WR + 40 * (lane & 3) + pbase;
  const int xr = (w & 3) * 32 + tok;
  const int offX = xr * 128 + 16 * (g ^ ((xr >> 1) & 7));
  v2u wf[NT][2]; v4u xf[2][2];
  LAS char *aN, *aP, *aX0, *aX1;
  auto base = [&](int bo) { aN = L + offN + bo; aP = L + offP + bo; aX0 = L + offX + bo; aX1 = L + (offX ^ 64) + bo; asm volatile("" : "+v"(aN), "+v"(aP), "+v"(aX0), "+v"(aX1)); };
  auto rd = [&](int s, int t, int h) { wf[t][h] = trd((t < NP ? aP + t * 8 : aN + (t - NP) * 32) + s * 512 + h * 16 * WR); };
  auto rdx = [&](int s, int a) { xf[s][a] = *(LAS v4u*)((s ? aX1 : aX0) + a * 2048); };
  auto mm = [&](int s, int t, int a, bool first = false) {
    const v4u f = {wf[t][0].x, wf[t][0].y, wf[t][1].x, wf[t][1].y};
    if (first) asm volatile("v_mfma_f32_16x16x32_bf16 %0, %1, %2, 0" : "=&v"(acc[a][t]) : "v"(f), "v"(xf[s][a]));
    else asm volatile("v_mfma_f32_16x16x32_bf16 %0, %1, %2, %0" : "+v"(acc[a][t]) : "v"(f), "v"(xf[s][a]));
  };
  constexpr int PR1 = 4, PR2 = 8, PR3 = NT + 4;
  auto prio = [&](int n) { if (n == 0) __builtin_amdgcn_s_setprio(3); else if (n == PR1) __builtin_amdgcn_s_setprio(2); else if (n == PR2) __builtin_amdgcn_s_setprio(1); else if (n == PR3) __builtin_amdgcn_s_setprio(0); };
  auto k1b = [&](auto&& hook) {
#pragma unroll
    for (int t = 2; t < NT; ++t) {
      prio(t - 2);
      mm(1, t, 0);
      if (t == 2) { rdx(0, 0); rdx(0, 1); } else if (t == 3) { rd(0, 1, 0); rd(0, 1, 1); } else rd(0, t - 1, 0);
      mm(1, t, 1);
      if (t == 2) { rd(0, 0, 0); rd(0, 0, 1); } else if (t == 3) { rd(0, 2, 0); rd(0, 2, 1); } else rd(0, t - 1, 1);
      hook(t - 2);
    }
  };
  auto k0 = [&](auto&& hook, bool first = false) {
#pragma unroll
    for (int t = 0; t < NT; ++t) {
      prio(NT - 2 + t);
      mm(0, t, 0, first);
      if (t) rd(1, t - 1, 0); else if (!first) rd(0, NT - 1, 0);
      if (t == 1 || t == 2) rdx(1, t - 1);
      mm(0, t, 1, first);
      if (t) rd(1, t - 1, 1); else if (!first) rd(0, NT - 1, 1);
      hook(NT - 2 + t);
    }
    mm(1, 0, 0); rd(1, NT - 1, 0); mm(1, 0, 1); rd(1, NT - 1, 1);
  };
  auto close = [&]() { mm(1, 1, 0); mm(1, 1, 1); WAITL0(); BAR(); };
  int bo = 0, dx = 2 * BUF + w * 2048, dw = 2 * BUF + XB + w * 4 * WR;
  auto ring = [&](int n) { if (n < 12 && !(n & 1)) piece(c, n >> 1, dx, dw); };
  base(0);
  rdx(0, 0); rdx(0, 1);
#pragma unroll
  for (int t = 0; t < NT; ++t) { rd(0, t, 0); rd(0, t, 1); }
  k0([&](int n) { if (n - (NT - 2) < 6) piece(c, n - (NT - 2), dx, dw); }, true);
  {
    LAS float* q = (LAS float*)(L + L_CP) + lane;
    float v = ((q[0] + q[64]) + (q[128] + q[192])) + q[256];
    if (!ISB && w == 3) st8(c.rw, c.b < 228 ? WS_CS + ((c.b / 57) * NCOL + (c.b % 57) * 64 + lane) * 8 : 0xfffffff0u, v2u{__builtin_bit_cast(unsigned, v), c.tag});
  }
  {
    const v4u e = {~0u, ~0u, ~0u, ~0u};
    const unsigned ot = WS_AFR + (c.tb * 16 + 2 * w) * NKS * 1024, kq = c.cg < 8 ? WS_KVP + ((c.cg * 4 + (w & 3)) * 16 + c.panel) * KVB + (w >> 2) * 1680 : OOB;
    st16(c.rw, ot + (lane >> 5) * NKS * 1024 + (54 + (c.head >> 1)) * 1024 + (c.head & 1) * 512 + (lane & 31) * 16, e);
    st16(c.rw, kq + lane * 16, e);
    st16(c.rw, sel(lane < 41, kq + 1024 + lane * 16, sel(lane >= 48, ot + ((lane >> 3) & 1) * NKS * 1024 + (70 + (c.head >> 3)) * 1024 + (c.head & 7) * 128 + (lane & 7) * 16, OOB)), e);
  }
  if (ISB) WAITV(10); else if (w < 3) WAITV(14); else WAITV(11);
  const unsigned fo = FL_PT + lane * 16;
  const unsigned po = w == 0 && lane == 0 ? FL_PT + c.b * 4 : OOB;
  v4u fl = {c.tag, c.tag, c.tag, c.tag};
#pragma unroll 1
  for (int s = 0; s < 7; ++s) {
    dx += bo - (bo == 0 ? 2 * BUF : bo - BUF); dw += bo - (bo == 0 ? 2 * BUF : bo - BUF);
    bo = bo == 2 * BUF ? 0 : bo + BUF;
    base(bo);
    close();
    k1b([&](int n) {
      if (n == 0) asm volatile("s_cmp_lg_u32 %4, 2\n\ts_cbranch_scc1 1f\n\tbuffer_store_dword %5, %2, %3, 0 offen sc1\n1:\ts_cmp_lg_u32 %4, 6\n\ts_cbranch_scc1 2f\n\tbuffer_load_dwordx4 %0, %1, %3, 0 offen sc1\n2:"
                               : "+v"(fl) : "v"(fo), "v"(po), "s"(c.rw), "s"(s), "v"(c.tag) : "memory", "scc");
      ring(n);
    });
    k0(ring);
    WAITV(6);
  }
  const unsigned wo = WS_WT + (c.ct * 5 * NKS + (ISB ? 38 + 4 * w : 14 * w - (w == 3) + c.rot)) * 1024 + lane * 16;
  constexpr int NW8 = ISB ? 10 : 2, NW9 = ISB ? 10 : 3;
  auto wl = [&](int n) { wpre[n / 5][n % 5] = ld16(c.rw, wo + ((n % 5) * NKS + n / 5) * 1024); };
  asm volatile("" : "+v"(fl));
#pragma unroll 1
  for (unsigned i = 0; i < SPINS && __builtin_amdgcn_ballot_w64(fl.x != c.tag || fl.y != c.tag || fl.z != c.tag || fl.w != c.tag); ++i) { asm volatile("" ::: "memory"); fl = ld16(c.rw, fo); }
  if (!ISB) st4(c.rw, c.b == 0 && w == 0 && lane == 0 ? WS_EPOCH : OOB, c.tag);
  base(2 * BUF);
  close();
  {
    const int tid = w * 64 + lane;
    const int col = img_col(c.cg, (tid >> 3) & 31) + (tid & 7);
    const unsigned ro = WS_RS + (c.panel * 128 + xr) * 8, co = WS_CS + col * 8;
    v2u r0, r1, c0, c1, c2, c3; unsigned bb;
    auto stat = [&]() {
      asm volatile("buffer_load_dwordx2 %0, %2, %3, 0 offen sc1\n\tbuffer_load_dwordx2 %1, %2, %3, 0 offen offset:128 sc1" : "=&v"(r0), "=&v"(r1) : "v"(ro), "s"(c.rw) : "memory");
      asm volatile("buffer_load_dwordx2 %0, %4, %5, 0 offen sc1\n\tbuffer_load_dwordx2 %1, %4, %5, %6 offen sc1\n\tbuffer_load_dwordx2 %2, %4, %5, %7 offen sc1\n\tbuffer_load_dwordx2 %3, %4, %5, %8 offen sc1"
                   : "=&v"(c0), "=&v"(c1), "=&v"(c2), "=&v"(c3) : "v"(ISB ? OOB : co), "s"(c.rw), "s"(NCOL * 8), "s"(2 * NCOL * 8), "s"(3 * NCOL * 8) : "memory");
    };
    k1b([&](int n) {
      if (n == 0) { stat(); asm volatile("global_load_ushort %0, %1, %2" : "=v"(bb) : "v"((col >= 3 * D ? col - 3 * D : 0) * 2), "s"(p.b1) : "memory"); }
      else if (n <= NW8) wl(n - 1);
    });
    k0([&](int n) { if (n <= NW8) wl(n - 1); });
    asm volatile("s_waitcnt vmcnt(%7)" : "+v"(r0), "+v"(r1), "+v"(c0), "+v"(c1), "+v"(c2), "+v"(c3), "+v"(bb) : "n"(NW8) : "memory");
    if (__builtin_amdgcn_ballot_w64(r0.y != c.tag || r1.y != c.tag || (!ISB && (c0.y != c.tag || c1.y != c.tag || c2.y != c.tag || c3.y != c.tag)))) {
#pragma unroll 1
      for (unsigned i = 0; i < SPINS; ++i) {
        stat();
        asm volatile("s_waitcnt vmcnt(0)" : "+v"(r0), "+v"(r1), "+v"(c0), "+v"(c1), "+v"(c2), "+v"(c3)::"memory");
        if (!__builtin_amdgcn_ballot_w64(r0.y != c.tag || r1.y != c.tag || (!ISB && (c0.y != c.tag || c1.y != c.tag || c2.y != c.tag || c3.y != c.tag)))) break;
      }
    }
    mb[0] = -lo(r0.x); rs[0] = __builtin_amdgcn_rcpf(hi(r0.x)); mb[1] = -lo(r1.x); rs[1] = __builtin_amdgcn_rcpf(hi(r1.x));
    asm volatile("" : "+v"(mb[0]), "+v"(mb[1]), "+v"(rs[0]), "+v"(rs[1]));
    if (!ISB) {
      *(LAS float*)(L + L_CS + tid * 4) = (__builtin_bit_cast(float, c0.x) + __builtin_bit_cast(float, c1.x)) + (__builtin_bit_cast(float, c2.x) + __builtin_bit_cast(float, c3.x));
      *(LAS float*)(L + L_B1 + tid * 4) = lo(bb);
    }
    base(0);
    close();
  }
  {
    k1b([&](int n) { if (n < NW9) wl(NW8 + n); });
    k0([&](int n) { if (n < NW9) wl(NW8 + n); });
#pragma unroll
    for (int t = 1; t < NT; ++t) { mm(1, t, 0); mm(1, t, 1); tail(t - 1); }
    __builtin_amdgcn_s_setprio(ISB ? 1 : 3);
    asm volatile("s_nop 15" ::: "memory");
    SB();
  }
}
DI void hconst(Ctx& c, int pos, f32x4 (&k)[4]) {
  LAS char* t = c.L + pos * 2 + (c.lane >> 4) * 16;
  k[0] = *(LAS f32x4*)(t + L_CS); k[1] = *(LAS f32x4*)(t + L_CS + 64); k[2] = *(LAS f32x4*)(t + L_B1); k[3] = *(LAS f32x4*)(t + L_B1 + 64);
}
DI void hidden(Ctx& c, const f32x4& a00, const f32x4& a01, const f32x4& a10, const f32x4& a11, const f32x4 (&k)[4], unsigned dst, const float (&mb)[2], const float (&rs)[2]) {
#pragma unroll
  for (int a = 0; a < 2; ++a) {
    const float n = mb[a] * rs[a];
    f32x4 u = (a ? a10 : a00) * rs[a] + (k[0] * n + k[2]), v = (a ? a11 : a01) * rs[a] + (k[1] * n + k[3]);
#pragma unroll
    for (int i = 0; i < 4; ++i) { u[i] *= __builtin_fmaxf(u[i], 0.f); v[i] *= __builtin_fmaxf(v[i], 0.f); }
    st16(c.rw, dst + a * NKS * 1024, pk8(u, v));
  }
}

#define K1MAP 1
#define K2MAP 1
template <bool ISB>
DI void run(const P& p, const int w) {
  extern __shared__ __attribute__((aligned(16))) char smem[];
  LAS char* L = (LAS char*)smem;
  Ctx c;
  const int b = blockIdx.x, xcd = b & 7, r = b >> 3;
  if (K1MAP == 0) { c.panel = xcd * 2 + (r >> 4); c.cg = r & 15; }
  else if (K1MAP == 1) { c.panel = (xcd >> 1) * 4 + (r >> 3); c.cg = (r & 3) + (xcd & 1) * 4 + ((r >> 2) & 1) * 8; }
  else { c.panel = (xcd >> 2) * 8 + (r >> 2); c.cg = (r & 1) + (xcd & 3) * 2 + ((r >> 1) & 1) * 8; }
  if (K2MAP == 0) { c.rt = xcd * 4 + (r >> 3); c.ct = r & 7; }
  else { c.rt = (xcd >> 1) * 8 + (r >> 2); c.ct = (xcd & 1) * 4 + (r & 3); }
  const int tid = threadIdx.x, lane = tid & 63;
  const int g = lane >> 4, tok = lane & 15, panel = c.panel, cg = c.cg, pid = panel * 16 + cg;
  const bool KV = cg < 8;
  c.L = L; c.b = b; c.w = w; c.lane = lane;
  c.head = 4 * xcd + (r & 3); c.tb = r >> 2;
  c.rot = 0;
  c.rw = __builtin_amdgcn_make_buffer_rsrc(p.ws, 0, WS_END, 0x00020000);
  unsigned ep; v4u xa, xc;
  {
    const char* row = (const char*)p.x + (size_t)(panel * 128 + cg * 8 + w) * (D * 2);
    asm volatile("global_load_dword %0, %1, %2 sc1" : "=v"(ep) : "v"(0u), "s"(p.ws) : "memory");
    asm volatile("global_load_dwordx4 %0, %1, %2" : "=v"(xa) : "v"(lane * 16), "s"(row) : "memory");
    asm volatile("global_load_dwordx4 %0, %1, %2 offset:1024" : "=v"(xc) : "v"(tok * 16), "s"(row) : "memory");
  }
  const int pks0 = 3 * (b / 10) + w, pks = pks0 < NKS ? pks0 : NKS - 1, pcb = b % 10;
  {
    const char* src; unsigned o0, st;
    if (!ISB && w < 3) {
      if (pks < 54) { src = (const char*)p.w2 + (size_t)(32 * pks) * (D * 2); o0 = (lane >> 3) * (D * 2); st = 8 * D * 2; }
      else if (pks < 70) { src = (const char*)p.wo + (size_t)(40 * (pks - 54)) * (D * 2); o0 = ((lane >> 5) * 8 + ((lane >> 3) & 3)) * (D * 2); st = 20 * D * 2; }
      else { src = (const char*)p.wo + (size_t)(160 * (pks - 70) + 16) * (D * 2); o0 = ((lane >> 5) * 40 + ((lane >> 3) & 3)) * (D * 2); st = 80 * D * 2; }
      src += pcb * 128;
    } else {
      const int jb = b < 228 ? b : 0, lc = jb % 57, k0 = (jb / 57) * 160 + (w - 3) * 32;
      if (lc < 30) { src = (const char*)p.wqkv + (size_t)k0 * (3 * D * 2) + lc * 128; st = 8 * 3 * D * 2; }
      else { src = (const char*)p.w1 + (size_t)k0 * (F * 2) + (lc - 30) * 128; st = 8 * F * 2; }
      o0 = (lane >> 3) * (st >> 3);
    }
    o0 += (lane & 7) * 16;
    const unsigned st2 = (!ISB && w < 3 && pks >= 54) ? (pks < 70 ? 4 * D * 2 : 20 * D * 2) : 2 * st;
    const unsigned dst = 2 * BUF + w * 4096;
    gldso(src, o0, dst); gldso(src, o0 + st, dst + 1024); gldso(src, o0 + st2, dst + 2048); gldso(src, o0 + st2 + st, dst + 3072);
  }
  {
    const int col = img_col(cg, lane & 31), k = (lane >> 5) * 32 + (w & 3) * 8 + (w >> 2) * 4;
    const bool f1 = col >= 3 * D;
    const unsigned pitch = f1 ? F * 2 : 3 * D * 2;
    const char* q = (f1 ? (const char*)p.w1 - 3 * D * 2 : (const char*)p.wqkv) + (k * pitch + col * 2);
    c.wadv = 64 * pitch;
#pragma unroll
    for (int i = 0; i < 4; ++i) c.wp[i] = q + i * pitch - c.wadv;
    c.xb = (const char*)p.x + (size_t)(panel * 128 + w * 16) * (D * 2);
    c.xo0 = (lane >> 3) * (D * 2) + 16 * ((lane & 7) ^ (lane >> 4)); c.xo1 = (8 + (lane >> 3)) * (D * 2) + 16 * ((lane & 7) ^ (4 | (lane >> 4)));
  }
#pragma unroll
  for (int i = 0; i < 6; ++i) piece(c, i, w * 2048, XB + w * 4 * WR);
  if (lane < 25) gldso((const char*)p.wphi + (cg & 7) * 3200 + w * 400, lane * 16, L_WPHI + w * 400);
#pragma unroll
  for (int i = 0; i < 6; ++i) piece(c, i, BUF + w * 2048, BUF + XB + w * 4 * WR);
  if (ISB && w == 7) *(LAS v4u*)(L + L_ZERO + lane * 16) = v4u{0, 0, 0, 0};
  if (!ISB && tid < 16) *(LAS unsigned*)(L + L_CNT + tid * 4) = 0;
  asm volatile("s_waitcnt vmcnt(17)" : "+v"(ep), "+v"(xa), "+v"(xc)::"memory");
  c.tag = __builtin_amdgcn_readfirstlane(ep) + 1u;
  unsigned stat;
  {
    float v[16];
#pragma unroll
    for (int i = 0; i < 4; ++i) { v[2 * i] = lo(xa[i]); v[2 * i + 1] = hi(xa[i]); v[8 + 2 * i] = lo(xc[i]); v[9 + 2 * i] = hi(xc[i]); }
    float s0 = 0.f, s1 = 0.f;
#pragma unroll
    for (int i = 0; i < 8; ++i) { s0 += v[i]; s1 += v[8 + i]; }
    const float m = wave_sum(lane < 16 ? s0 + s1 : s0) * (1.f / D);
    s0 = 0.f; s1 = 0.f;
#pragma unroll
    for (int i = 0; i < 8; ++i) { s0 += (v[i] - m) * (v[i] - m); s1 += (v[8 + i] - m) * (v[8 + i] - m); }
    const float var = wave_sum(lane < 16 ? s0 + s1 : s0) * (1.f / D);
    stat = pk(m, __builtin_sqrtf(rb(rb(var) + 1e-5f)));
  }
  WAITV(13);
  v4u sf[4];
#pragma unroll
  for (int t = 0; t < 4; ++t) sf[t] = trd2(L + 2 * BUF + w * 4096 + (lane >> 2) * 128 + (lane & 3) * 8 + t * 32, 16 * 128);
  if (ISB || w == 3) {
    const v4u ones = {0x3f803f80u, 0x3f803f80u, 0x3f803f80u, 0x3f803f80u};
#pragma unroll
    for (int t = 0; t < 4; ++t) {
      f32x4 cs = mfma32(sf[t], ones, f32x4{0, 0, 0, 0});
      if (tok == 0) *(LAS f32x4*)(L + L_CP + (w - 3) * 256 + t * 64 + g * 16) = cs;
    }
  }
  WAITV(7);
  st8(c.rw, lane == 0 ? WS_RS + (panel * 128 + cg * 8 + w) * 8 : 0xfffffff0u, v2u{stat, c.tag});
  if (!ISB && w < 3) {
#pragma unroll
    for (int t = 0; t < 4; ++t) st16(c.rw, pks0 < NKS && b < 250 ? WS_WT + ((4 * pcb + t) * NKS + pks) * 1024 + lane * 16 : 0xffffffe0u, sf[t]);
  }
  WAITL0(); BAR(); SB();

  v4u wpre[6][5];
  float mb[2], rs[2];
  const int dd = (cg & 1) * 32, rt = c.rt, ct = c.ct, aw = w & 3;
  const unsigned afr = WS_AFR + rt * 4 * NKS * 1024 + lane * 16, wt = WS_WT + ct * 5 * NKS * 1024 + lane * 16;
  const unsigned fh = sel(lane < 16, MB_H + (panel * 16 + lane) * 256 + cg * 4, OOB);
  const unsigned hd0 = WS_AFR + (panel * 8 + aw * 2) * NKS * 1024 + lane * 16;
  auto pack = [&](f32x4 (&v)[7], v4u (&bq)[3]) {
    bq[0] = pk8(v[0], v[1]); bq[1] = pk8(v[2], v[3]);
    const v2u e = pk4(v[4]); bq[2] = v4u{e.x, e.y, 0x00003f80u, 0u};
  };
  if (!ISB) {
    if (!KV) {
      f32x4 acc[2][8], hk[4][4];
      unsigned hd, hl;
      gemm1<0, 8, false>(p, c, 0, 256, acc, wpre, mb, rs, [&](int n) {
        if (n < 4) hconst(c, 256 + 64 * n, hk[n]);
        if (n == 4) { hd = hd0 + (cg < 15 ? 5 * cg - 23 : 51) * 1024; hl = cg < 14 ? hd : OOB; asm volatile("" : "+v"(hd), "+v"(hl)); }
      });
#pragma unroll
      for (int u = 0; u < 4; ++u) hidden(c, acc[0][2 * u], acc[0][2 * u + 1], acc[1][2 * u], acc[1][2 * u + 1], hk[u], (u < 3 ? hd : hl) + u * 1024, mb, rs);
    } else {
      f32x4 acc[2][7], hk[4], cs[5];
      unsigned hd; LAS char* d;
      gemm1<5, 2, false>(p, c, 192 + dd, 384, acc, wpre, mb, rs, [&](int n) {
        if (n == 0) hconst(c, 384, hk);
        if (n == 1) {
#pragma unroll
          for (int t = 0; t < 5; ++t) cs[t] = *(LAS f32x4*)(L + L_CS + (192 + dd) * 2 + g * 80 + t * 16);
        }
        if (n == 2) { hd = hd0 + 2 * cg * 1024; d = L + L_V + (w * 32 + tok) * PV + g * 48; asm volatile("" : "+v"(hd), "+v"(d)); }
      });
      hidden(c, acc[0][5], acc[0][6], acc[1][5], acc[1][6], hk, hd, mb, rs);
      SB();
#pragma unroll
      for (int a = 0; a < 2; ++a) {
        v4u bq[3];
        const float n = mb[a] * rs[a];
#pragma unroll
        for (int t = 0; t < 5; ++t) acc[a][t] = acc[a][t] * rs[a] + cs[t] * n;
        pack(acc[a], bq);
        *(LAS v4u*)(d + a * 16 * PV) = bq[0]; *(LAS v4u*)(d + a * 16 * PV + 16) = bq[1]; *(LAS v4u*)(d + a * 16 * PV + 32) = bq[2];
      }
      arrive(C_V);
    }
    constexpr int NK = 14;
    const unsigned ap = afr + (14 * w - (w == 3)) * 1024, bp = wt + (14 * w - (w == 3)) * 1024, apl = w < 2 ? ap : OOB, bpl = w < 2 ? bp : OOB;
    auto pld = [&](unsigned off, unsigned so) { SB(); asm volatile("s_waitcnt vmcnt(%0)" ::"n"(INF - 1)); const v4u v = ld16s(c.rw, off, so); SB(); return v; };
    const int nk = w < 2 ? 14 : 13;
    auto ko = [&](int k) { const int kk = c.rot + k; return (kk >= nk ? kk - nk : kk) * 1024; };
    auto wld = [&](int n) { return pld(n / 5 < NK - 1 ? bp : bpl, (n % 5) * NKS * 1024 + ko(n / 5)); };
    SB();
    __builtin_amdgcn_s_setprio(0);
#pragma unroll
    for (int n = 5; n < 8; ++n) wpre[n / 5][n % 5] = ld16s(c.rw, bp, (n % 5) * NKS * 1024 + ko(n / 5));
    SB();
    WAITV(3);
    arrive_flag(C_H, 7, c.rw, fh, c.tag);
#pragma unroll
    for (int n = 8; n < 30; ++n) wpre[n / 5][n % 5] = wld(n);
    LAS char* stg = L + w * NST * 1024 + lane * 16;
    f32x4 acc[4][5];
    {
      v4u t[NST]; v2u xv[5], bv[5];
#pragma unroll
      for (int n = 0; n < NST; ++n) t[n] = wld(30 + n);
#pragma unroll
      for (int j = 0; j < 5; ++j) {
        xv[j] = *(const v2u*)((const char*)p.x + (size_t)(rt * 64 + w * 16 + tok) * (D * 2) + ct * 160 + j * 32 + g * 8);
        bv[j] = *(const v2u*)((const char*)p.b2 + ct * 160 + j * 32 + g * 8);
      }
      SB();
      await(L, C_H, 8);
#pragma unroll
      for (int n = 0; n < NST; ++n) *(LAS v4u*)(stg + n * 1024) = t[n];
#pragma unroll
      for (int j = 0; j < 5; ++j) acc[3][j] = f32x4{lo(xv[j].x) + lo(bv[j].x), hi(xv[j].x) + hi(bv[j].x), lo(xv[j].y) + lo(bv[j].y), hi(xv[j].y) + hi(bv[j].y)};
      asm volatile("" : "+v"(acc[3][0]), "+v"(acc[3][1]), "+v"(acc[3][2]), "+v"(acc[3][3]), "+v"(acc[3][4]));
      SB();
    }
    wait_flags<16>(p.ws, c.rw, MB_H + (rt * 8 + ct) * 256, 0, c.tag);
    v4u ra[RA];
    unsigned so[4];
#pragma unroll
    for (int i = 0; i < 4; ++i) so[i] = ((w + 1 + i) & 3) * NKS * 1024;
    auto ald = [&](int m) { return pld(m / 4 < NK - 1 ? ap : apl, so[m % 4] + ko(m / 4)); };
#pragma unroll
    for (int m = 0; m < RA; ++m) ra[m] = ald(m);
#pragma unroll
    for (int k = 0; k < NK; ++k) {
#pragma unroll
      for (int i = 0; i < 4; ++i) {
#pragma unroll
        for (int j = 0; j < 5; ++j) {
          acc[i][j] = mfma32(wpre[k % 6][j], ra[(k * 4 + i) % RA], k || i == 3 ? acc[i][j] : f32x4{0, 0, 0, 0});
          const int n = i * 5 + j, f = (k + 5) * 5 + n / 4;
          if (k && k + 5 < NK && n % 4 == 1) {
            if (f < 30 + NST) { wpre[(k + 5) % 6][n / 4] = *(LAS v4u*)(stg + (f - 30) * 1024); SB(); }
            else wpre[(k + 5) % 6][n / 4] = wld(f);
          }
          if (k == NK - 1 && i) { *(LAS f32x4*)(stg + ((i - 1) * 5 + j) * 1024) = acc[i - 1][j]; SB(); }
        }
        if (k * 4 + i + RA < NK * 4) ra[(k * 4 + i) % RA] = ald(k * 4 + i + RA);
      }
      SB();
    }
    arrive(C_A);
    await(L, C_A, 4);
    LAS char* q0 = L + ((w + 3) & 3) * NST * 1024 + lane * 16;
    LAS char* q1 = L + ((w + 2) & 3) * NST * 1024 + 5120 + lane * 16;
    LAS char* q2 = L + ((w + 1) & 3) * NST * 1024 + 10240 + lane * 16;
#pragma unroll
    for (int j = 0; j < 5; ++j) acc[3][j] = (acc[3][j] + *(LAS f32x4*)(q0 + j * 1024)) + (*(LAS f32x4*)(q1 + j * 1024) + *(LAS f32x4*)(q2 + j * 1024));
#pragma unroll
    for (int j = 0; j < 5; ++j) *(LAS f32x4*)(q0 + j * 1024) = acc[3][j];
    arrive(C_X + 4 * w);
  } else {
    const int head = c.head, tb = c.tb;
    {
      f32x4 acc[2][7], pq[2][5], hk[4], cs[5];
      unsigned hd; LAS char* wp;
      gemm1<5, 2, true>(p, c, dd, KV ? 448 : 192, acc, wpre, mb, rs, [&](int n) {
        if (n == 0) hconst(c, KV ? 448 : 192, hk);
        if (n == 1) {
#pragma unroll
          for (int t = 0; t < 5; ++t) cs[t] = *(LAS f32x4*)(L + L_CS + dd * 2 + g * 80 + t * 16);
        }
        if (n == 2) {
          hd = hd0 + (KV ? 2 * cg + 1 : (cg < 15 ? 5 * cg - 24 : 50)) * 1024;
          wp = L + sel((lane & 3) == g, L_WPHI + g * 800 + ((lane & 15) >> 2) * 40, L_ZERO);
          asm volatile("" : "+v"(hd), "+v"(wp));
        }
      });
      {
        v4u a0[5], a1[5], bq[2][3]; v2u a2[5];
#pragma unroll
        for (int t = 0; t < 5; ++t) a0[t] = trd2(wp + t * 8, 160);
        hidden(c, acc[0][5], acc[0][6], acc[1][5], acc[1][6], hk, hd, mb, rs);
        SB();
#pragma unroll
        for (int t = 0; t < 5; ++t) { a1[t] = trd2(wp + 320 + t * 8, 160); a2[t] = trd(wp + 640 + t * 8); }
#pragma unroll
        for (int a = 0; a < 2; ++a) {
#pragma unroll
          for (int t = 0; t < 5; ++t) acc[a][t] += cs[t] * mb[a];
          pack(acc[a], bq[a]);
        }
        SB();
#pragma unroll
        for (int a = 0; a < 2; ++a) {
#pragma unroll
          for (int t = 0; t < 5; ++t) pq[a][t] = mfma32(a0[t], bq[a][0], f32x4{0, 0, 0, 0});
#pragma unroll
          for (int t = 0; t < 5; ++t) pq[a][t] = mfma32(a1[t], bq[a][1], pq[a][t]);
#pragma unroll
          for (int t = 0; t < 5; ++t) pq[a][t] = mfma32(v4u{a2[t].x, a2[t].y, 0u, 0u}, bq[a][2], pq[a][t]);
        }
      }
      SB();
      WAITV(0);
      arrive_flag(C_H, 7, c.rw, fh, c.tag);
#pragma unroll
      for (int a = 0; a < 2; ++a) {
        unsigned wv[20];
        dual_softmax(pq[a], rs[a], wv, [&]() {});
        const int tt = aw * 2 + a;
        if (KV) {
          LAS char* d = L + L_PHK + (tt * 16 + tok) * PK + g * 80;
          *(LAS v4u*)(d) = v4u{wv[0], wv[1], wv[4], wv[5]};
          *(LAS v4u*)(d + 16) = v4u{wv[8], wv[9], wv[12], wv[13]};
          *(LAS v4u*)(d + 32) = v4u{wv[2], wv[3], wv[6], wv[7]};
          *(LAS v4u*)(d + 48) = v4u{wv[10], wv[11], wv[14], wv[15]};
          *(LAS v4u*)(d + 64) = v4u{wv[16], wv[17], wv[18], wv[19]};
        } else {
          const unsigned d = WS_PHQ + ((panel * 8 + tt) * 32 + (cg - 8) * 4 + g) * 1280 + tok * 16;
#pragma unroll
          for (int i = 0; i < 5; ++i) st16(c.rw, d + i * 256, v4u{wv[4 * i], wv[4 * i + 1], wv[4 * i + 2], wv[4 * i + 3]});
        }
        SB();
      }
    }
    if (KV) {
      arrive(C_V);
      await(L, C_V, 8);
      f32x4 kv[2][3];
      LAS char* A = L + L_PHK + (lane >> 2) * PK + aw * 80 + (lane & 3) * 8;
      LAS char* Bv = L + L_V + (lane >> 2) * PV + aw * 48 + (lane & 3) * 16;
#pragma unroll
      for (int s = 0; s < 4; ++s) {
        const v4u b0 = trd2(Bv + s * 32 * PV, 16 * PV), b1 = trd2(Bv + 8 + s * 32 * PV, 16 * PV);
#pragma unroll
        for (int T = 0; T < 3; ++T) {
          const v4u f = trd2(A + s * 32 * PK + T * 32, 16 * PK);
          kv[0][T] = mfma32(f, b0, s ? kv[0][T] : f32x4{0, 0, 0, 0}); kv[1][T] = mfma32(f, b1, s ? kv[1][T] : f32x4{0, 0, 0, 0});
        }
      }
      const unsigned d = WS_KVP + ((cg * 4 + aw) * 16 + panel) * KVB;
      const unsigned d0 = tok < 12 ? d + (g * 12 + tok) * 16 : OOB, d1 = tok < 9 ? d + (g * 9 + tok) * 16 : OOB;
#pragma unroll
      for (int T = 0; T < 3; ++T) {
        st16(c.rw, (T < 2 || lane < 32 ? d0 : OOB) + T * 1344, __builtin_bit_cast(v4u, kv[0][T]));
        st16(c.rw, (T < 2 || lane < 32 ? d1 : OOB) + T * 1344 + (T < 2 ? 768 : 384), __builtin_bit_cast(v4u, kv[1][T]));
      }
      SB();
      st4(c.rw, sel(lane < 8, MB_K + ((cg * 4 + aw) * 8 + lane) * 128 + panel * 4, OOB), c.tag);
    }
#pragma unroll
    for (int j = 0; j < 5; ++j) wpre[4][j] = ld16(c.rw, wt + (j * NKS + 70 + aw) * 1024);
    SB();
    if (!KV) {
      WAITV(5);
      arrive_flag(C_Q, 3, c.rw, sel(lane < 4, MB_Q + (((cg - 8) * 4 + lane) * 8 + (panel >> 1)) * 128 + (panel & 1) * 4, OOB), c.tag);
    }
    v16u q0; v8u q1;
    {
      const int ch = aw * 56 + lane, T = (ch >= 84) + (ch >= 168), r0 = ch - 84 * T, n0 = T < 2 ? 48 : 24, dt = r0 >= n0, r1 = r0 - dt * n0;
      const int kg = (r1 * (dt ? 57 : 43)) >> 9, kc = r1 - kg * (dt ? 9 : 12);
      const bool on = lane < 56 && ch < 210;
      const unsigned ko = on ? WS_KVP + head * 16 * KVB + ch * 16 : OOB;
      const unsigned qo = WS_PHQ + ((tb * 16 + aw * 4) * 32 + head) * 1280, qo1 = lane < 32 ? qo + 1024 + tok * 16 + g * 8 : OOB;
      int kw = L_KV + sel(!on, 2560 + lane * 8, sel(T < 2, ((dt * 4 + kg) * 16 + kc) * 16 + T * 8, 2048 + ((dt * 2 + kg) * 16 + kc) * 8));
      asm volatile("" : "+v"(kw));
      f32x32 ka, kb; unsigned t0, t1, st, it; wait_flags<17>(p.ws, c.rw, MB_K + (head * 8 + tb) * 128, MB_Q + (head * 8 + tb) * 128 + (aw >> 1) * 4, c.tag);
      asm volatile(
        "s_mov_b32 %[st], 0\n\t" "buffer_load_dwordx4 v[192:195], %[ko], %[rw], %[st] offen sc1\n\t" "buffer_load_dwordx4 v[196:199], %[ko], %[rw], %[st] offen offset:3456 sc1\n\t"
        "s_mov_b32 %[st], 6912\n\t" "buffer_load_dwordx4 v[200:203], %[ko], %[rw], %[st] offen sc1\n\t" "buffer_load_dwordx4 v[204:207], %[ko], %[rw], %[st] offen offset:3456 sc1\n\t"
        "s_mov_b32 %[st], 13824\n\t" "buffer_load_dwordx4 v[208:211], %[ko], %[rw], %[st] offen sc1\n\t" "buffer_load_dwordx4 v[212:215], %[ko], %[rw], %[st] offen offset:3456 sc1\n\t"
        "s_mov_b32 %[st], 20736\n\t" "buffer_load_dwordx4 v[216:219], %[ko], %[rw], %[st] offen sc1\n\t" "buffer_load_dwordx4 v[220:223], %[ko], %[rw], %[st] offen offset:3456 sc1\n\t"
        "s_mov_b32 %[st], 27648\n\t" "buffer_load_dwordx4 v[224:227], %[ko], %[rw], %[st] offen sc1\n\t" "buffer_load_dwordx4 v[228:231], %[ko], %[rw], %[st] offen offset:3456 sc1\n\t"
        "s_mov_b32 %[st], 34560\n\t" "buffer_load_dwordx4 v[232:235], %[ko], %[rw], %[st] offen sc1\n\t" "buffer_load_dwordx4 v[236:239], %[ko], %[rw], %[st] offen offset:3456 sc1\n\t"
        "s_mov_b32 %[st], 41472\n\t" "buffer_load_dwordx4 v[240:243], %[ko], %[rw], %[st] offen sc1\n\t" "buffer_load_dwordx4 v[244:247], %[ko], %[rw], %[st] offen offset:3456 sc1\n\t"
        "s_mov_b32 %[st], 48384\n\t" "buffer_load_dwordx4 v[248:251], %[ko], %[rw], %[st] offen sc1\n\t" "buffer_load_dwordx4 v[252:255], %[ko], %[rw], %[st] offen offset:3456 sc1\n\t"
        "s_mov_b32 %[st], 0\n\t" "buffer_load_dwordx4 v[168:171], %[qa], %[rw], %[st] offen sc1\n\t" "buffer_load_dwordx2 v[184:185], %[qb], %[rw], %[st] offen sc1\n\t"
        "s_mov_b32 %[st], 40960\n\t" "buffer_load_dwordx4 v[172:175], %[qa], %[rw], %[st] offen sc1\n\t" "buffer_load_dwordx2 v[186:187], %[qb], %[rw], %[st] offen sc1\n\t"
        "s_mov_b32 %[st], 81920\n\t" "buffer_load_dwordx4 v[176:179], %[qa], %[rw], %[st] offen sc1\n\t" "buffer_load_dwordx2 v[188:189], %[qb], %[rw], %[st] offen sc1\n\t"
        "s_mov_b32 %[st], 122880\n\t" "buffer_load_dwordx4 v[180:183], %[qa], %[rw], %[st] offen sc1\n\t" "buffer_load_dwordx2 v[190:191], %[qb], %[rw], %[st] offen sc1\n\t"
        "s_mov_b32 %[it], %[sp]\n\t" "s_waitcnt vmcnt(8)\n\t" "1:\tv_max3_u32 %[t0], v192, v193, v194\n\t" "v_max3_u32 %[t1], v195, v196, v197\n\t" "v_max3_u32 %[t0], %[t0], v198, v199\n\t"
        "v_max3_u32 %[t1], %[t1], v200, v201\n\t" "v_max3_u32 %[t0], %[t0], v202, v203\n\t" "v_max3_u32 %[t1], %[t1], v204, v205\n\t" "v_max3_u32 %[t0], %[t0], v206, v207\n\t"
        "v_max3_u32 %[t1], %[t1], v208, v209\n\t" "v_max3_u32 %[t0], %[t0], v210, v211\n\t" "v_max3_u32 %[t1], %[t1], v212, v213\n\t" "v_max3_u32 %[t0], %[t0], v214, v215\n\t"
        "v_max3_u32 %[t1], %[t1], v216, v217\n\t" "v_max3_u32 %[t0], %[t0], v218, v219\n\t" "v_max3_u32 %[t1], %[t1], v220, v221\n\t" "v_max3_u32 %[t0], %[t0], v222, v223\n\t"
        "v_max3_u32 %[t1], %[t1], v224, v225\n\t" "v_max3_u32 %[t0], %[t0], v226, v227\n\t" "v_max3_u32 %[t1], %[t1], v228, v229\n\t" "v_max3_u32 %[t0], %[t0], v230, v231\n\t"
        "v_max3_u32 %[t1], %[t1], v232, v233\n\t" "v_max3_u32 %[t0], %[t0], v234, v235\n\t" "v_max3_u32 %[t1], %[t1], v236, v237\n\t" "v_max3_u32 %[t0], %[t0], v238, v239\n\t"
        "v_max3_u32 %[t1], %[t1], v240, v241\n\t" "v_max3_u32 %[t0], %[t0], v242, v243\n\t" "v_max3_u32 %[t1], %[t1], v244, v245\n\t" "v_max3_u32 %[t0], %[t0], v246, v247\n\t"
        "v_max3_u32 %[t1], %[t1], v248, v249\n\t" "v_max3_u32 %[t0], %[t0], v250, v251\n\t" "v_max3_u32 %[t1], %[t1], v252, v253\n\t" "v_max3_u32 %[t0], %[t0], v254, v255\n\t"
        "v_max_u32 %[t0], %[t0], %[t1]\n\t" "v_cmp_eq_u32 vcc, -1, %[t0]\n\t" "s_cbranch_vccz 2f\n\t" "s_mov_b32 %[st], 0\n\t" "buffer_load_dwordx4 v[192:195], %[ko], %[rw], %[st] offen sc1\n\t"
        "buffer_load_dwordx4 v[196:199], %[ko], %[rw], %[st] offen offset:3456 sc1\n\t" "s_mov_b32 %[st], 6912\n\t" "buffer_load_dwordx4 v[200:203], %[ko], %[rw], %[st] offen sc1\n\t"
        "buffer_load_dwordx4 v[204:207], %[ko], %[rw], %[st] offen offset:3456 sc1\n\t" "s_mov_b32 %[st], 13824\n\t" "buffer_load_dwordx4 v[208:211], %[ko], %[rw], %[st] offen sc1\n\t"
        "buffer_load_dwordx4 v[212:215], %[ko], %[rw], %[st] offen offset:3456 sc1\n\t" "s_mov_b32 %[st], 20736\n\t" "buffer_load_dwordx4 v[216:219], %[ko], %[rw], %[st] offen sc1\n\t"
        "buffer_load_dwordx4 v[220:223], %[ko], %[rw], %[st] offen offset:3456 sc1\n\t" "s_mov_b32 %[st], 27648\n\t" "buffer_load_dwordx4 v[224:227], %[ko], %[rw], %[st] offen sc1\n\t"
        "buffer_load_dwordx4 v[228:231], %[ko], %[rw], %[st] offen offset:3456 sc1\n\t" "s_mov_b32 %[st], 34560\n\t" "buffer_load_dwordx4 v[232:235], %[ko], %[rw], %[st] offen sc1\n\t"
        "buffer_load_dwordx4 v[236:239], %[ko], %[rw], %[st] offen offset:3456 sc1\n\t" "s_mov_b32 %[st], 41472\n\t" "buffer_load_dwordx4 v[240:243], %[ko], %[rw], %[st] offen sc1\n\t"
        "buffer_load_dwordx4 v[244:247], %[ko], %[rw], %[st] offen offset:3456 sc1\n\t" "s_mov_b32 %[st], 48384\n\t" "buffer_load_dwordx4 v[248:251], %[ko], %[rw], %[st] offen sc1\n\t"
        "buffer_load_dwordx4 v[252:255], %[ko], %[rw], %[st] offen offset:3456 sc1\n\t" "s_waitcnt vmcnt(0)\n\t" "s_sub_u32 %[it], %[it], 1\n\t" "s_cbranch_scc0 1b\n\t" "2:"
        : "=&{v[192:223]}"(ka), "=&{v[224:255]}"(kb), "=&{v[168:183]}"(q0), "=&{v[184:191]}"(q1), [t0] "=&v"(t0), [t1] "=&v"(t1), [st] "=&s"(st), [it] "=&s"(it)
        : [ko] "v"(ko), [qa] "v"(qo + lane * 16), [qb] "v"(qo1), [rw] "s"(c.rw), [sp] "n"(SPINS) : "memory", "vcc", "scc");
      ka += kb;
      const f32x16 kc16 = ka.lo + ka.hi;
      const f32x8 kc8 = kc16.lo + kc16.hi;
      *(LAS v2u*)(L + kw) = pk4(kc8.lo + kc8.hi);
    }
    arrive(C_K);
    await(L, C_K, 4);
    {
      const int c0 = tok < 12 ? tok : 0, c1 = tok < 8 ? tok : 8;
      const v4u A00 = *(LAS v4u*)(L + L_KV + (g * 16 + c0) * 16), A01 = *(LAS v4u*)(L + L_KV + 1024 + (g * 16 + c1) * 16);
      const v2u A10 = *(LAS v2u*)(L + L_KV + 2048 + ((g & 1) * 16 + c0) * 8), A11 = *(LAS v2u*)(L + L_KV + 2304 + ((g & 1) * 16 + c1) * 8);
      asm volatile("s_waitcnt vmcnt(0)" : "+{v[168:183]}"(q0), "+{v[184:191]}"(q1)::"memory");
      const unsigned ob = WS_AFR + (tb * 16 + aw * 4) * NKS * 1024;
      const unsigned oa = sel(lane < 32, ob + (54 + (head >> 1)) * 1024 + (head & 1) * 512 + lane * 16, OOB), oc = sel(g == 2, ob + (70 + (head >> 3)) * 1024 + (head & 7) * 128 + tok * 8, OOB);
      f32x4 o0[4], o1[4];
#pragma unroll
      for (int i = 0; i < 4; ++i) { o1[i] = mfma32(A01, sub4(q0, i), f32x4{0, 0, 0, 0}); o0[i] = mfma32(A00, sub4(q0, i), f32x4{0, 0, 0, 0}); }
#pragma unroll
      for (int i = 0; i < 4; ++i) { const v2u e = {q1[2 * i], q1[2 * i + 1]}; o1[i] = mfma16(A11, e, o1[i]); o0[i] = mfma16(A10, e, o0[i]); }
#pragma unroll
      for (int i = 0; i < 4; ++i) {
        float u = o1[i][0] + 1e-6f, den = u;
        asm volatile("s_nop 1\n\tv_permlane32_swap_b32 %0, %1" : "+v"(u), "+v"(den));
        const float rd = __builtin_amdgcn_rcpf(den);
        const v4u v = pk8(o0[i] * rd, o1[i] * rd);
        st16(c.rw, oa + i * NKS * 1024, v);
        st8(c.rw, oc + i * NKS * 1024, v2u{v.x, v.y});
      }
      SB();
      st4(c.rw, sel(lane < 8, MB_O + ((tb * 4 + aw) * 8 + lane) * 256 + head * 4, OOB), c.tag);
    }
    {
      v16u of[4]; v4u og[4]; unsigned t0, t1, it;
      const unsigned fo = afr + (54 + 4 * aw) * 1024, ro = WS_AFR + (rt * 4 * NKS + 70 + aw) * 1024 + g * 256 + tok * 8;
      unsigned so[4];
#pragma unroll
      for (int n = 0; n < 4; ++n) so[n] = ((aw + 1 + n) & 3) * NKS * 1024;
      wait_flags<8>(p.ws, c.rw, MB_O + (rt * 8 + ct) * 256 + aw * 32, 0, c.tag);
      __builtin_amdgcn_s_waitcnt(0x0f70);
      SB();
      O_ISSUE0(); O_ISSUE1(); O_ISSUE2(); O_ISSUE3();
      f32x4 acc[2][5], t[5];
      unsigned cb, cx;
#pragma unroll
      for (int n = 0; n < 4; ++n) {
        if (n == 0) O_GET0(); else if (n == 1) O_GET1(); else if (n == 2) O_GET2(); else if (n == 3) O_GET3();
#pragma unroll
        for (int k = 0; k < 5; ++k) {
#pragma unroll
          for (int j = 0; j < 5; ++j) {
            acc[n & 1][j] = mfma32(wpre[k][j], k < 4 ? sub4(of[n], k) : og[n], k ? acc[n & 1][j] : f32x4{0, 0, 0, 0});
            if (n && k == 0) { *(LAS f32x4*)(L + L_XB + (((aw + n) & 3) * 3 + n - 1) * 5120 + j * 1024 + lane * 16) = acc[~n & 1][j]; SB(); }
          }
          if (n == 3 && k == 0) arrive(C_B);
          if (n == 3 && k == 1) { cb = peekv(L, C_B); cx = peekv(L, C_X + 4 * aw); }
          if (n == 3 && k == 2) {
            if (__builtin_amdgcn_readfirstlane(cb) < 4) await(L, C_B, 4);
            if (__builtin_amdgcn_readfirstlane(cx) < 1) await(L, C_X + 4 * aw, 1);
            asm volatile("" ::: "memory");
#pragma unroll
            for (int j = 0; j < 5; ++j) {
              LAS char* q = L + L_XB + aw * 15360 + j * 1024 + lane * 16;
              t[j] = (*(LAS f32x4*)q + *(LAS f32x4*)(q + 5120)) + (*(LAS f32x4*)(q + 10240) + *(LAS f32x4*)(L + ((aw + 3) & 3) * NST * 1024 + j * 1024 + lane * 16));
            }
          } else if (n < 3 || k < 2) SB();
        }
      }
#pragma unroll
      for (int j = 0; j < 5; ++j) *(v2u*)((char*)p.out + (size_t)(rt * 64 + aw * 16 + tok) * (D * 2) + ct * 160 + j * 32 + g * 8) = pk4(acc[1][j] + t[j]);
    }
  }
}
__global__ __launch_bounds__(512, 2) void fk(P p) {
  const int w = __builtin_amdgcn_readfirstlane(threadIdx.x >> 6);
  if (w < 4) run<false>(p, w); else run<true>(p, w);
}
}

extern "C" void kernel_launch(void* const* d_in, const int* in_sizes, int n_in, void* d_out, int out_size, void* d_ws, size_t ws_size, hipStream_t stream) {
  using namespace hk;
  P p;
  p.x = (const u16*)d_in[0]; p.wqkv = (const u16*)d_in[1]; p.wphi = (const u16*)d_in[2]; p.wo = (const u16*)d_in[3];
  p.w1 = (const u16*)d_in[5]; p.b1 = (const u16*)d_in[6]; p.w2 = (const u16*)d_in[7]; p.b2 = (const u16*)d_in[8];
  p.out = (u16*)d_out; p.ws = (char*)d_ws;
  (void)hipFuncSetAttribute((const void*)fk, hipFuncAttributeMaxDynamicSharedMemorySize, L_BYTES);
  fk<<<256, 512, L_BYTES, stream>>>(p);
}
```

```cpp
#define NOROT
#include <hip/hip_runtime.h>
#include <hip/hip_bf16.h>
#include <stdint.h>

#define GAS __attribute__((address_space(1)))
#define LAS __attribute__((address_space(3)))
#define DI __device__ __forceinline__
typedef unsigned short u16;
typedef unsigned long long u64;
typedef unsigned v4u __attribute__((ext_vector_type(4)));
typedef unsigned v2u __attribute__((ext_vector_type(2)));
typedef float f32x4 __attribute__((ext_vector_type(4)));
typedef unsigned v8u __attribute__((ext_vector_type(8)));
typedef unsigned v16u __attribute__((ext_vector_type(16)));
typedef float f32x8 __attribute__((ext_vector_type(8)));
typedef float f32x16 __attribute__((ext_vector_type(16)));
typedef float f32x32 __attribute__((ext_vector_type(32)));
typedef float f32x2 __attribute__((ext_vector_type(2)));
typedef short s16x4 __attribute__((ext_vector_type(4)));
typedef short s16x8 __attribute__((ext_vector_type(8)));
typedef __bf16 bf16x2 __attribute__((ext_vector_type(2)));

namespace hk {
constexpr int S = 2048, D = 640, F = 1728, NKS = 74;
constexpr int NCOL = 3 * D + F;
constexpr unsigned WS_EPOCH = 0;
constexpr unsigned WS_FLAG = 4096;
constexpr unsigned FL_PT = WS_FLAG;
constexpr unsigned MB_H = WS_FLAG + 4096, MB_O = MB_H + 128;
constexpr unsigned MB_K = MB_H + 65536, MB_Q = MB_K + 64;
constexpr unsigned WS_RS = MB_K + 32768;
constexpr unsigned WS_CS = WS_RS + S * 8;
constexpr unsigned WS_AFR = 262144;
constexpr unsigned WS_WT = WS_AFR + 128 * NKS * 1024;
constexpr unsigned WS_PHQ = WS_WT + 40 * NKS * 1024;
constexpr unsigned KVB = 3456;
constexpr unsigned WS_KVP = WS_PHQ + 128 * 32 * 1280;
constexpr unsigned WS_END = WS_KVP + 32 * 16 * KVB;
static_assert(WS_CS + 4 * NCOL * 8 <= WS_AFR, "ws");
constexpr int XB = 16384, WR = 1056, BUF = XB + 32 * WR;
constexpr int L_WPHI = 3 * BUF, L_ZERO = L_WPHI + 3328, L_CS = L_ZERO + 1024, L_B1 = L_CS + 1024, L_CP = L_B1 + 1024, L_CNT = L_CP + 1280, L_KV = L_CNT + 64, L_BYTES = L_KV + 3072 + 1024;
constexpr int PK = 352, PV = 224;
constexpr int L_V = 3 * BUF - 128 * PV - 64, L_PHK = L_V - 128 * PK - 64;
constexpr int NST = 18, L_XB = L_PHK;
constexpr int C_H = L_CNT, C_K = L_CNT + 4, C_V = L_CNT + 8, C_Q = L_CNT + 12, C_A = L_CNT + 16, C_B = L_CNT + 20, C_X = L_CNT + 24;
constexpr unsigned OOB = 0xf0000000u;
static_assert(L_PHK >= BUF && 4 * NST * 1024 <= L_PHK && L_XB + 61440 <= 3 * BUF && L_BYTES <= 163840, "lds");

DI f32x4 mfma32(v4u a, v4u b, f32x4 c) { return __builtin_amdgcn_mfma_f32_16x16x32_bf16(__builtin_bit_cast(s16x8, a), __builtin_bit_cast(s16x8, b), c, 0, 0, 0); }
DI f32x4 mfma16(v2u a, v2u b, f32x4 c) { return mfma32(v4u{a.x, a.y, 0u, 0u}, v4u{b.x, b.y, 0u, 0u}, c); }
DI v2u trd(LAS char* p) { return __builtin_bit_cast(v2u, __builtin_amdgcn_ds_read_tr16_b64_v4i16((LAS s16x4*)p)); }
DI v4u trd2(LAS char* p, int d) { v2u a = trd(p), b = trd(p + d); return v4u{a.x, a.y, b.x, b.y}; }
DI unsigned pk(float a, float b) { f32x2 v = {a, b}; return __builtin_bit_cast(unsigned, __builtin_convertvector(v, bf16x2)); }
DI v2u pk4(f32x4 a) { return v2u{pk(a[0], a[1]), pk(a[2], a[3])}; }
DI v4u pk8(f32x4 a, f32x4 b) { return v4u{pk(a[0], a[1]), pk(a[2], a[3]), pk(b[0], b[1]), pk(b[2], b[3])}; }
DI float lo(unsigned u) { return __builtin_bit_cast(float, u << 16); }
DI float hi(unsigned u) { return __builtin_bit_cast(float, u & 0xffff0000u); }
DI float rb(float a) { return lo(pk(a, a)); }
DI void glds(const void* g, unsigned dst) { asm volatile("s_mov_b32 m0, %1\n\ts_nop 0\n\tglobal_load_lds_dwordx4 %0, off" ::"v"(g), "s"(dst) : "memory"); }
DI void gldso(const void* base, unsigned off, unsigned dst) { asm volatile("s_mov_b32 m0, %2\n\ts_nop 0\n\tglobal_load_lds_dwordx4 %0, %1" ::"v"(off), "s"(base), "s"(dst) : "memory"); }
#define BAR() __builtin_amdgcn_s_barrier()
#define WAITV(n) asm volatile("s_waitcnt vmcnt(" #n ")" ::: "memory")
#define WAITL0() asm volatile("s_waitcnt lgkmcnt(0)" ::: "memory")
#define SB() __builtin_amdgcn_sched_barrier(0)
typedef __amdgpu_buffer_rsrc_t rsrc_t;
DI v4u ld16(rsrc_t r, unsigned off) { return __builtin_amdgcn_raw_buffer_load_b128(r, off, 0, 16); }
DI v4u ld16s(rsrc_t r, unsigned off, unsigned so) { return __builtin_amdgcn_raw_buffer_load_b128(r, off, so, 16); }
DI v2u ld8(rsrc_t r, unsigned off) { return __builtin_amdgcn_raw_buffer_load_b64(r, off, 0, 16); }
DI unsigned ld4(rsrc_t r, unsigned off) { return __builtin_amdgcn_raw_buffer_load_b32(r, off, 0, 16); }
DI void st16(rsrc_t r, unsigned off, v4u v) { __builtin_amdgcn_raw_buffer_store_b128(v, r, off, 0, 16); }
DI void st8(rsrc_t r, unsigned off, v2u v) { __builtin_amdgcn_raw_buffer_store_b64(v, r, off, 0, 16); }
DI void st4(rsrc_t r, unsigned off, unsigned v) { __builtin_amdgcn_raw_buffer_store_b32(v, r, off, 0, 16); }
constexpr unsigned SPINS = 1u << 16;
#define INF 12
#define FLEAD 8
#define RA 8
#define K1MAP 1
#define K2MAP 1
DI void wait_tail(rsrc_t r, unsigned off, unsigned tag) {
#pragma unroll 1
  for (unsigned i = 0; i < SPINS; ++i) {
    asm volatile("" ::: "memory");
    if (__builtin_amdgcn_ballot_w64(ld4(r, off) != tag) == 0) break;
  }
}
template <int N, class V> DI unsigned differs(const V& v, unsigned tag) {
  u64 a = ~0ull, o = 0;
#pragma unroll
  for (int i = 0; i < N; i += 2) { const u64 x = (u64)v[i + 1] << 32 | v[i]; a &= x; o |= x; }
  unsigned l = (unsigned)a & (unsigned)(a >> 32), h = (unsigned)o | (unsigned)(o >> 32);
  asm("" : "+s"(l), "+s"(h));
  return (l ^ tag) | (h ^ tag);
}
template <int N> DI void wait_flags(const char* ws, rsrc_t r, unsigned off, unsigned off1, unsigned tag) {
  unsigned d = 1;
#pragma unroll 1
  for (unsigned i = 0; i < 4096 && d; ++i) {
    if (N == 8) { v8u v; asm volatile("s_load_dwordx8 %0, %1, %2 glc\n\ts_waitcnt lgkmcnt(0)" : "=s"(v) : "s"(ws), "s"(off) : "memory"); d = differs<8>(v, tag); }
    else if (N == 16) { v16u v; asm volatile("s_load_dwordx16 %0, %1, %2 glc\n\ts_waitcnt lgkmcnt(0)" : "=s"(v) : "s"(ws), "s"(off) : "memory"); d = differs<16>(v, tag); }
    else { v16u v; unsigned u; asm volatile("s_load_dwordx16 %0, %2, %3 glc\n\ts_load_dword %1, %2, %4 glc\n\ts_waitcnt lgkmcnt(0)" : "=&s"(v), "=&s"(u) : "s"(ws), "s"(off), "s"(off1) : "memory"); d = differs<16>(v, tag) | (u ^ tag); }
  }
  if (d) { const unsigned l = threadIdx.x & 63; wait_tail(r, N == 17 && l == 16 ? off1 : off + (l & (N == 8 ? 7 : 15)) * 4, tag); }
}
DI void arrive(int c) { asm volatile("s_mov_b64 exec, 1\n\tds_add_u32 %0, %1\n\ts_mov_b64 exec, -1" ::"v"(c), "v"(1u) : "memory"); }
DI void arrive_flag(int c, unsigned last, rsrc_t r, unsigned off, unsigned tag) {
  unsigned t, u;
  asm volatile("s_mov_b64 exec, 1\n\tds_add_rtn_u32 %0, %2, %3\n\ts_mov_b64 exec, -1\n\ts_waitcnt lgkmcnt(0)\n\tv_readfirstlane_b32 %1, %0\n\ts_cmp_lg_u32 %1, %4\n\ts_cbranch_scc1 1f\n\tbuffer_store_dword %5, %6, %7, 0 offen sc1\n1:"
               : "=&v"(t), "=&s"(u) : "v"(c), "v"(1u), "n"(last), "v"(tag), "v"(off), "s"(r) : "memory", "scc");
}
DI unsigned peekv(LAS char* L, int c) { return __hip_atomic_load((LAS unsigned*)(L + c), __ATOMIC_RELAXED, __HIP_MEMORY_SCOPE_WORKGROUP); }
DI unsigned peek(LAS char* L, int c) { return __builtin_amdgcn_readfirstlane(peekv(L, c)); }
DI void await(LAS char* L, int c, unsigned n) {
  #pragma unroll 1
  for (unsigned i = 0; i < (1u << 22) && peek(L, c) < n; ++i) __builtin_amdgcn_s_sleep(1);
  asm volatile("" ::: "memory");
}
DI unsigned sel(bool c, unsigned a, unsigned b) { return c ? a : b; }
DI v4u sub4(const v16u& v, int i) { return v4u{v[4 * i], v[4 * i + 1], v[4 * i + 2], v[4 * i + 3]}; }
#define O_ISSUE0() asm volatile( \
        "buffer_load_dwordx4 v[176:179], %[fo], %[rw], %[so] offen sc1\n\t" "buffer_load_dwordx4 v[180:183], %[fo], %[rw], %[so] offen offset:1024 sc1\n\t"  \
        "buffer_load_dwordx4 v[184:187], %[fo], %[rw], %[so] offen offset:2048 sc1\n\t" "buffer_load_dwordx4 v[188:191], %[fo], %[rw], %[so] offen offset:3072 sc1\n\t"  \
        "buffer_load_dwordx2 v[192:193], %[ro], %[rw], %[so] offen sc1\n\t" "buffer_load_dwordx2 v[194:195], %[ro], %[rw], %[so] offen offset:128 sc1\n\t" "" \
        : "=&{v[176:191]}"(of[0]), "=&{v[192:195]}"(og[0]) : [fo] "v"(fo), [ro] "v"(ro), [rw] "s"(c.rw), [so] "s"(so[0]) : "memory")
#define O_GET0() asm volatile( \
        "s_mov_b32 %[it], %[sp]\n\t" "s_waitcnt vmcnt(18)\n\t" "1:\tv_max3_u32 %[t0], v176, v177, v178\n\t" "v_max3_u32 %[t1], v179, v180, v181\n\t" "v_max3_u32 %[t0], %[t0], v182, v183\n\t"  \
        "v_max3_u32 %[t1], %[t1], v184, v185\n\t" "v_max3_u32 %[t0], %[t0], v186, v187\n\t" "v_max3_u32 %[t1], %[t1], v188, v189\n\t" "v_max3_u32 %[t0], %[t0], v190, v191\n\t"  \
        "v_max3_u32 %[t1], %[t1], v192, v193\n\t" "v_max3_u32 %[t0], %[t0], v194, v195\n\t" "v_max_u32 %[t0], %[t0], %[t1]\n\t" "v_cmp_eq_u32 vcc, -1, %[t0]\n\t" "s_cbranch_vccz 2f\n\t"  \
        "buffer_load_dwordx4 v[176:179], %[fo], %[rw], %[so] offen sc1\n\t" "buffer_load_dwordx4 v[180:183], %[fo], %[rw], %[so] offen offset:1024 sc1\n\t"  \
        "buffer_load_dwordx4 v[184:187], %[fo], %[rw], %[so] offen offset:2048 sc1\n\t" "buffer_load_dwordx4 v[188:191], %[fo], %[rw], %[so] offen offset:3072 sc1\n\t"  \
        "buffer_load_dwordx2 v[192:193], %[ro], %[rw], %[so] offen sc1\n\t" "buffer_load_dwordx2 v[194:195], %[ro], %[rw], %[so] offen offset:128 sc1\n\t" "s_waitcnt vmcnt(0)\n\t"  \
        "s_sub_u32 %[it], %[it], 1\n\t" "s_cbranch_scc0 1b\n\t" "2:" \
        : "+{v[176:191]}"(of[0]), "+{v[192:195]}"(og[0]), [t0] "=&v"(t0), [t1] "=&v"(t1), [it] "=&s"(it) \
        : [fo] "v"(fo), [ro] "v"(ro), [rw] "s"(c.rw), [so] "s"(so[0]), [sp] "n"(SPINS) : "memory", "vcc", "scc")
#define O_ISSUE1() asm volatile( \
        "buffer_load_dwordx4 v[196:199], %[fo], %[rw], %[so] offen sc1\n\t" "buffer_load_dwordx4 v[200:203], %[fo], %[rw], %[so] offen offset:1024 sc1\n\t"  \
        "buffer_load_dwordx4 v[204:207], %[fo], %[rw], %[so] offen offset:2048 sc1\n\t" "buffer_load_dwordx4 v[208:211], %[fo], %[rw], %[so] offen offset:3072 sc1\n\t"  \
        "buffer_load_dwordx2 v[212:213], %[ro], %[rw], %[so] offen sc1\n\t" "buffer_load_dwordx2 v[214:215], %[ro], %[rw], %[so] offen offset:128 sc1\n\t" "" \
        : "=&{v[196:211]}"(of[1]), "=&{v[212:215]}"(og[1]) : [fo] "v"(fo), [ro] "v"(ro), [rw] "s"(c.rw), [so] "s"(so[1]) : "memory")
#define O_GET1() asm volatile( \
        "s_mov_b32 %[it], %[sp]\n\t" "s_waitcnt vmcnt(12)\n\t" "1:\tv_max3_u32 %[t0], v196, v197, v198\n\t" "v_max3_u32 %[t1], v199, v200, v201\n\t" "v_max3_u32 %[t0], %[t0], v202, v203\n\t"  \
        "v_max3_u32 %[t1], %[t1], v204, v205\n\t" "v_max3_u32 %[t0], %[t0], v206, v207\n\t" "v_max3_u32 %[t1], %[t1], v208, v209\n\t" "v_max3_u32 %[t0], %[t0], v210, v211\n\t"  \
        "v_max3_u32 %[t1], %[t1], v212, v213\n\t" "v_max3_u32 %[t0], %[t0], v214, v215\n\t" "v_max_u32 %[t0], %[t0], %[t1]\n\t" "v_cmp_eq_u32 vcc, -1, %[t0]\n\t" "s_cbranch_vccz 2f\n\t"  \
        "buffer_load_dwordx4 v[196:199], %[fo], %[rw], %[so] offen sc1\n\t" "buffer_load_dwordx4 v[200:203], %[fo], %[rw], %[so] offen offset:1024 sc1\n\t"  \
        "buffer_load_dwordx4 v[204:207], %[fo], %[rw], %[so] offen offset:2048 sc1\n\t" "buffer_load_dwordx4 v[208:211], %[fo], %[rw], %[so] offen offset:3072 sc1\n\t"  \
        "buffer_load_dwordx2 v[212:213], %[ro], %[rw], %[so] offen sc1\n\t" "buffer_load_dwordx2 v[214:215], %[ro], %[rw], %[so] offen offset:128 sc1\n\t" "s_waitcnt vmcnt(0)\n\t"  \
        "s_sub_u32 %[it], %[it], 1\n\t" "s_cbranch_scc0 1b\n\t" "2:" \
        : "+{v[196:211]}"(of[1]), "+{v[212:215]}"(og[1]), [t0] "=&v"(t0), [t1] "=&v"(t1), [it] "=&s"(it) \
        : [fo] "v"(fo), [ro] "v"(ro), [rw] "s"(c.rw), [so] "s"(so[1]), [sp] "n"(SPINS) : "memory", "vcc", "scc")
#define O_ISSUE2() asm volatile( \
        "buffer_load_dwordx4 v[216:219], %[fo], %[rw], %[so] offen sc1\n\t" "buffer_load_dwordx4 v[220:223], %[fo], %[rw], %[so] offen offset:1024 sc1\n\t"  \
        "buffer_load_dwordx4 v[224:227], %[fo], %[rw], %[so] offen offset:2048 sc1\n\t" "buffer_load_dwordx4 v[228:231], %[fo], %[rw], %[so] offen offset:3072 sc1\n\t"  \
        "buffer_load_dwordx2 v[232:233], %[ro], %[rw], %[so] offen sc1\n\t" "buffer_load_dwordx2 v[234:235], %[ro], %[rw], %[so] offen offset:128 sc1\n\t" "" \
        : "=&{v[216:231]}"(of[2]), "=&{v[232:235]}"(og[2]) : [fo] "v"(fo), [ro] "v"(ro), [rw] "s"(c.rw), [so] "s"(so[2]) : "memory")
#define O_GET2() asm volatile( \
        "s_mov_b32 %[it], %[sp]\n\t" "s_waitcnt vmcnt(6)\n\t" "1:\tv_max3_u32 %[t0], v216, v217, v218\n\t" "v_max3_u32 %[t1], v219, v220, v221\n\t" "v_max3_u32 %[t0], %[t0], v222, v223\n\t"  \
        "v_max3_u32 %[t1], %[t1], v224, v225\n\t" "v_max3_u32 %[t0], %[t0], v226, v227\n\t" "v_max3_u32 %[t1], %[t1], v228, v229\n\t" "v_max3_u32 %[t0], %[t0], v230, v231\n\t"  \
        "v_max3_u32 %[t1], %[t1], v232, v233\n\t" "v_max3_u32 %[t0], %[t0], v234, v235\n\t" "v_max_u32 %[t0], %[t0], %[t1]\n\t" "v_cmp_eq_u32 vcc, -1, %[t0]\n\t" "s_cbranch_vccz 2f\n\t"  \
        "buffer_load_dwordx4 v[216:219], %[fo], %[rw], %[so] offen sc1\n\t" "buffer_load_dwordx4 v[220:223], %[fo], %[rw], %[so] offen offset:1024 sc1\n\t"  \
        "buffer_load_dwordx4 v[224:227], %[fo], %[rw], %[so] offen offset:2048 sc1\n\t" "buffer_load_dwordx4 v[228:231], %[fo], %[rw], %[so] offen offset:3072 sc1\n\t"  \
        "buffer_load_dwordx2 v[232:233], %[ro], %[rw], %[so] offen sc1\n\t" "buffer_load_dwordx2 v[234:235], %[ro], %[rw], %[so] offen offset:128 sc1\n\t" "s_waitcnt vmcnt(0)\n\t"  \
        "s_sub_u32 %[it], %[it], 1\n\t" "s_cbranch_scc0 1b\n\t" "2:" \
        : "+{v[216:231]}"(of[2]), "+{v[232:235]}"(og[2]), [t0] "=&v"(t0), [t1] "=&v"(t1), [it] "=&s"(it) \
        : [fo] "v"(fo), [ro] "v"(ro), [rw] "s"(c.rw), [so] "s"(so[2]), [sp] "n"(SPINS) : "memory", "vcc", "scc")
#define O_ISSUE3() asm volatile( \
        "buffer_load_dwordx4 v[236:239], %[fo], %[rw], %[so] offen sc1\n\t" "buffer_load_dwordx4 v[240:243], %[fo], %[rw], %[so] offen offset:1024 sc1\n\t"  \
        "buffer_load_dwordx4 v[244:247], %[fo], %[rw], %[so] offen offset:2048 sc1\n\t" "buffer_load_dwordx4 v[248:251], %[fo], %[rw], %[so] offen offset:3072 sc1\n\t"  \
        "buffer_load_dwordx2 v[252:253], %[ro], %[rw], %[so] offen sc1\n\t" "buffer_load_dwordx2 v[254:255], %[ro], %[rw], %[so] offen offset:128 sc1\n\t" "" \
        : "=&{v[236:251]}"(of[3]), "=&{v[252:255]}"(og[3]) : [fo] "v"(fo), [ro] "v"(ro), [rw] "s"(c.rw), [so] "s"(so[3]) : "memory")
#define O_GET3() asm volatile( \
        "s_mov_b32 %[it], %[sp]\n\t" "s_waitcnt vmcnt(0)\n\t" "1:\tv_max3_u32 %[t0], v236, v237, v238\n\t" "v_max3_u32 %[t1], v239, v240, v241\n\t" "v_max3_u32 %[t0], %[t0], v242, v243\n\t"  \
        "v_max3_u32 %[t1], %[t1], v244, v245\n\t" "v_max3_u32 %[t0], %[t0], v246, v247\n\t" "v_max3_u32 %[t1], %[t1], v248, v249\n\t" "v_max3_u32 %[t0], %[t0], v250, v251\n\t"  \
        "v_max3_u32 %[t1], %[t1], v252, v253\n\t" "v_max3_u32 %[t0], %[t0], v254, v255\n\t" "v_max_u32 %[t0], %[t0], %[t1]\n\t" "v_cmp_eq_u32 vcc, -1, %[t0]\n\t" "s_cbranch_vccz 2f\n\t"  \
        "buffer_load_dwordx4 v[236:239], %[fo], %[rw], %[so] offen sc1\n\t" "buffer_load_dwordx4 v[240:243], %[fo], %[rw], %[so] offen offset:1024 sc1\n\t"  \
        "buffer_load_dwordx4 v[244:247], %[fo], %[rw], %[so] offen offset:2048 sc1\n\t" "buffer_load_dwordx4 v[248:251], %[fo], %[rw], %[so] offen offset:3072 sc1\n\t"  \
        "buffer_load_dwordx2 v[252:253], %[ro], %[rw], %[so] offen sc1\n\t" "buffer_load_dwordx2 v[254:255], %[ro], %[rw], %[so] offen offset:128 sc1\n\t" "s_waitcnt vmcnt(0)\n\t"  \
        "s_sub_u32 %[it], %[it], 1\n\t" "s_cbranch_scc0 1b\n\t" "2:" \
        : "+{v[236:251]}"(of[3]), "+{v[252:255]}"(og[3]), [t0] "=&v"(t0), [t1] "=&v"(t1), [it] "=&s"(it) \
        : [fo] "v"(fo), [ro] "v"(ro), [rw] "s"(c.rw), [so] "s"(so[3]), [sp] "n"(SPINS) : "memory", "vcc", "scc")
template <int CTRL, int RM> DI float dpp_add(float v) { return v + __builtin_bit_cast(float, __builtin_amdgcn_update_dpp(0, __builtin_bit_cast(int, v), CTRL, RM, 0xf, false)); }
DI float wave_sum(float v) {
  v = dpp_add<0x111, 0xf>(v); v = dpp_add<0x112, 0xf>(v); v = dpp_add<0x114, 0xf>(v); v = dpp_add<0x118, 0xf>(v);
  v = dpp_add<0x142, 0xa>(v); v = dpp_add<0x143, 0xc>(v);
  return __builtin_bit_cast(float, __builtin_amdgcn_readlane(__builtin_bit_cast(int, v), 63));
}

struct P {
  const u16 *x, *wqkv, *wphi, *wo, *w1, *b1, *w2, *b2;
  u16* out;
  char* ws;
};

template <class F>
DI void dual_softmax(const f32x4 (&a)[5], float sc, unsigned (&w)[20], F&& mid) {
  float m0 = __builtin_fmaxf(a[0][0], a[0][1]), m1 = __builtin_fmaxf(a[0][2], a[0][3]), n0 = __builtin_fminf(a[0][0], a[0][1]), n1 = __builtin_fminf(a[0][2], a[0][3]);
#pragma unroll
  for (int i = 1; i < 5; ++i) {
    m0 = __builtin_fmaxf(__builtin_fmaxf(m0, a[i][0]), a[i][1]); m1 = __builtin_fmaxf(__builtin_fmaxf(m1, a[i][2]), a[i][3]);
    n0 = __builtin_fminf(__builtin_fminf(n0, a[i][0]), a[i][1]); n1 = __builtin_fminf(__builtin_fminf(n1, a[i][2]), a[i][3]);
  }
  const float L2E = 1.4426950408889634f * sc;
#pragma unroll
  for (int h = 0; h < 2; ++h) {
    const float c = h ? __builtin_fminf(n0, n1) * L2E : __builtin_fmaxf(m0, m1) * -L2E, k = h ? -L2E : L2E;
    f32x2 e[10];
#pragma unroll
    for (int i = 0; i < 10; ++i) e[i] = __builtin_elementwise_fma(f32x2{a[i >> 1][(i & 1) * 2], a[i >> 1][(i & 1) * 2 + 1]}, f32x2{k, k}, f32x2{c, c});
    SB();
#pragma unroll
    for (int i = 0; i < 10; ++i) e[i] = f32x2{__builtin_amdgcn_exp2f(e[i].x), __builtin_amdgcn_exp2f(e[i].y)};
    SB();
    const f32x2 s = (((e[0] + e[1]) + (e[2] + e[3])) + ((e[4] + e[5]) + (e[6] + e[7]))) + (e[8] + e[9]);
    const float r = __builtin_amdgcn_rcpf(s.x + s.y);
#pragma unroll
    for (int i = 0; i < 10; ++i) { const f32x2 v = e[i] * f32x2{r, r}; w[10 * h + i] = pk(v.x, v.y); }
    if (h == 0) { SB(); mid(); SB(); }
  }
}

struct Ctx {
  LAS char* L; rsrc_t rw; unsigned tag;
  int b, panel, cg, w, lane, rt, ct, head, tb, rot;
  const char* wp[4]; u64 wadv; const char* xb; unsigned xo0, xo1;
};
DI void piece(Ctx& c, int i, int dx, int dw) {
  if (i < 2) {
    asm volatile("s_add_i32 m0, %2, %3\n\ts_nop 0\n\tglobal_load_lds_dwordx4 %0, %1" ::"v"(i ? c.xo1 : c.xo0), "s"(c.xb), "s"(dx), "n"(i * 1024) : "memory", "scc");
    if (i) c.xb += 128;
  } else {
    asm volatile("s_add_i32 m0, %2, %3\n\tv_lshl_add_u64 %0, %0, 0, %1\n\tglobal_load_lds_dwordx4 %0, off" : "+v"(c.wp[i - 2]) : "v"(c.wadv), "s"(dw), "n"((i - 2) * WR) : "memory", "scc");
  }
}
DI int img_col(int cg, int ch) {
  const int win = ((160 * (cg & 7)) & ~63) >> 1, j = cg - 8;
  const int b0 = cg < 8 ? D + win : win;
  const int b1 = cg < 8 ? 2 * D + win - 96 : 3 * D + (j < 7 ? 512 + 160 * j : 1600) - 96;
  const int b2 = cg < 8 ? 3 * D + 64 * cg - 192 : b1;
  const int cc = j >= 6 && ch > 27 ? 27 : ch;
  return (ch < 12 ? b0 : ch < 24 ? b1 : b2) + 8 * cc;
}

template <int NP, int NN, bool ISB, class TF>
DI void gemm1(const P& p, Ctx& c, int pbase, int nbase, f32x4 (&acc)[2][NP + NN], v4u (&wpre)[6][5], v2u (&xv)[10], float (&mb)[2], float (&rs)[2], TF&& tail) {
  constexpr int NT = NP + NN;
  LAS char* L = c.L;
  const int lane = c.lane, w = c.w, g = lane >> 4, tok = lane & 15;
  const int offN = XB + (lane >> 2) * WR + 8 * (lane & 3) + nbase, offP = XB + (lane >> 2) * WR + 40 * (lane & 3) + pbase;
  const int xr = (w & 3) * 32 + tok;
  const int offX = xr * 128 + 16 * (g ^ ((xr >> 1) & 7));
  v2u wf[NT][2]; v4u xf[2][2];
  LAS char *aN, *aP, *aX0, *aX1;
  auto base = [&](int bo) { aN = L + offN + bo; aP = L + offP + bo; aX0 = L + offX + bo; aX1 = L + (offX ^ 64) + bo; asm volatile("" : "+v"(aN), "+v"(aP), "+v"(aX0), "+v"(aX1)); };
  auto rd = [&](int s, int t, int h) { wf[t][h] = trd((t < NP ? aP + t * 8 : aN + (t - NP) * 32) + s * 512 + h * 16 * WR); };
  auto rdx = [&](int s, int a) { xf[s][a] = *(LAS v4u*)((s ? aX1 : aX0) + a * 2048); };
  auto mm = [&](int s, int t, int a, bool first = false) {
    const v4u f = {wf[t][0].x, wf[t][0].y, wf[t][1].x, wf[t][1].y};
    if (first) asm volatile("v_mfma_f32_16x16x32_bf16 %0, %1, %2, 0" : "=&v"(acc[a][t]) : "v"(f), "v"(xf[s][a]));
    else asm volatile("v_mfma_f32_16x16x32_bf16 %0, %1, %2, %0" : "+v"(acc[a][t]) : "v"(f), "v"(xf[s][a]));
  };
  constexpr int PR1 = 4, PR2 = 8, PR3 = NT + 4;
  auto prio = [&](int n) { if (n == 0) __builtin_amdgcn_s_setprio(3); else if (n == PR1) __builtin_amdgcn_s_setprio(2); else if (n == PR2) __builtin_amdgcn_s_setprio(1); else if (n == PR3) __builtin_amdgcn_s_setprio(0); };
  auto k1b = [&](auto&& hook) {
#pragma unroll
    for (int t = 2; t < NT; ++t) {
      prio(t - 2);
      mm(1, t, 0);
      if (t == 2) { rdx(0, 0); rdx(0, 1); } else if (t == 3) { rd(0, 1, 0); rd(0, 1, 1); } else rd(0, t - 1, 0);
      mm(1, t, 1);
      if (t == 2) { rd(0, 0, 0); rd(0, 0, 1); } else if (t == 3) { rd(0, 2, 0); rd(0, 2, 1); } else rd(0, t - 1, 1);
      hook(t - 2);
    }
  };
  auto k0 = [&](auto&& hook, bool first = false) {
#pragma unroll
    for (int t = 0; t < NT; ++t) {
      prio(NT - 2 + t);
      mm(0, t, 0, first);
      if (t) rd(1, t - 1, 0); else if (!first) rd(0, NT - 1, 0);
      if (t == 1 || t == 2) rdx(1, t - 1);
      mm(0, t, 1, first);
      if (t) rd(1, t - 1, 1); else if (!first) rd(0, NT - 1, 1);
      hook(NT - 2 + t);
    }
    mm(1, 0, 0); rd(1, NT - 1, 0); mm(1, 0, 1); rd(1, NT - 1, 1);
  };
  auto close = [&]() { mm(1, 1, 0); mm(1, 1, 1); WAITL0(); BAR(); };
  int bo = 0, dx = 2 * BUF + w * 2048, dw = 2 * BUF + XB + w * 4 * WR;
#define RH 0, 2, 4, 6, 8, 10
  constexpr int rh[6] = {RH};
  auto ring = [&](int n) {
#pragma unroll
    for (int i = 0; i < 6; ++i) if (n == rh[i]) piece(c, i, dx, dw);
  };
  base(0);
  rdx(0, 0); rdx(0, 1);
#pragma unroll
  for (int t = 0; t < NT; ++t) { rd(0, t, 0); rd(0, t, 1); }
  k0([&](int n) { if (n - (NT - 2) < 6) piece(c, n - (NT - 2), dx, dw); }, true);
  {
    LAS float* q = (LAS float*)(L + L_CP) + lane;
    float v = ((q[0] + q[64]) + (q[128] + q[192])) + q[256];
    if (!ISB && w == 3) st8(c.rw, c.b < 228 ? WS_CS + ((c.b / 57) * NCOL + (c.b % 57) * 64 + lane) * 8 : 0xfffffff0u, v2u{__builtin_bit_cast(unsigned, v), c.tag});
  }
  {
    const v4u e = {~0u, ~0u, ~0u, ~0u};
    const unsigned ot = WS_AFR + (c.tb * 16 + 2 * w) * NKS * 1024, kq = c.cg < 8 ? WS_KVP + ((c.cg * 4 + (w & 3)) * 16 + c.panel) * KVB + (w >> 2) * 1680 : OOB;
    st16(c.rw, ot + (lane >> 5) * NKS * 1024 + (54 + (c.head >> 1)) * 1024 + (c.head & 1) * 512 + (lane & 31) * 16, e);
    st16(c.rw, kq + lane * 16, e);
    st16(c.rw, sel(lane < 41, kq + 1024 + lane * 16, sel(lane >= 48, ot + ((lane >> 3) & 1) * NKS * 1024 + (70 + (c.head >> 3)) * 1024 + (c.head & 7) * 128 + (lane & 7) * 16, OOB)), e);
  }
  if (ISB) WAITV(10); else if (w < 3) WAITV(14); else WAITV(11);
  const unsigned fo = FL_PT + lane * 16;
  const unsigned po = w == 0 && lane == 0 ? FL_PT + c.b * 4 : OOB;
  v4u fl = {c.tag, c.tag, c.tag, c.tag};
#pragma unroll 1
  for (int s = 0; s < 7; ++s) {
    dx += bo - (bo == 0 ? 2 * BUF : bo - BUF); dw += bo - (bo == 0 ? 2 * BUF : bo - BUF);
    bo = bo == 2 * BUF ? 0 : bo + BUF;
    base(bo);
    close();
    k1b([&](int n) {
      if (n == 0) asm volatile("s_cmp_lg_u32 %4, 2\n\ts_cbranch_scc1 1f\n\tbuffer_store_dword %5, %2, %3, 0 offen sc1\n1:\ts_cmp_lg_u32 %4, 6\n\ts_cbranch_scc1 2f\n\tbuffer_load_dwordx4 %0, %1, %3, 0 offen sc1\n2:"
                               : "+v"(fl) : "v"(fo), "v"(po), "s"(c.rw), "s"(s), "v"(c.tag) : "memory", "scc");
      ring(n);
    });
    k0(ring);
    WAITV(6);
  }
  const unsigned wo = WS_WT + (c.ct * 5 * NKS + (ISB ? 38 + 4 * w : 14 * w - (w == 3) + c.rot)) * 1024 + lane * 16;
  constexpr int NW8 = ISB ? 10 : 2, NW9 = ISB ? 10 : 3;
  auto wl = [&](int n) { wpre[n / 5][n % 5] = ld16(c.rw, wo + ((n % 5) * NKS + n / 5) * 1024); };
  asm volatile("" : "+v"(fl));
#pragma unroll 1
  for (unsigned i = 0; i < SPINS && __builtin_amdgcn_ballot_w64(fl.x != c.tag || fl.y != c.tag || fl.z != c.tag || fl.w != c.tag); ++i) { asm volatile("" ::: "memory"); fl = ld16(c.rw, fo); }
  if (!ISB) st4(c.rw, c.b == 0 && w == 0 && lane == 0 ? WS_EPOCH : OOB, c.tag);
  base(2 * BUF);
  close();
  {
    const int tid = w * 64 + lane;
    const int col = img_col(c.cg, (tid >> 3) & 31) + (tid & 7);
    const unsigned ro = WS_RS + (c.panel * 128 + xr) * 8, co = WS_CS + col * 8;
    v2u r0, r1, c0, c1, c2, c3; unsigned bb;
    auto stat = [&]() {
      asm volatile("buffer_load_dwordx2 %0, %2, %3, 0 offen sc1\n\tbuffer_load_dwordx2 %1, %2, %3, 0 offen offset:128 sc1" : "=&v"(r0), "=&v"(r1) : "v"(ro), "s"(c.rw) : "memory");
      asm volatile("buffer_load_dwordx2 %0, %4, %5, 0 offen sc1\n\tbuffer_load_dwordx2 %1, %4, %5, %6 offen sc1\n\tbuffer_load_dwordx2 %2, %4, %5, %7 offen sc1\n\tbuffer_load_dwordx2 %3, %4, %5, %8 offen sc1"
                   : "=&v"(c0), "=&v"(c1), "=&v"(c2), "=&v"(c3) : "v"(ISB ? OOB : co), "s"(c.rw), "s"(NCOL * 8), "s"(2 * NCOL * 8), "s"(3 * NCOL * 8) : "memory");
    };
    k1b([&](int n) {
      if (n == 0) { stat(); asm volatile("global_load_ushort %0, %1, %2" : "=v"(bb) : "v"((col >= 3 * D ? col - 3 * D : 0) * 2), "s"(p.b1) : "memory"); }
      else if (n <= NW8) wl(n - 1);
    });
    k0([&](int n) { if (n <= NW8) wl(n - 1); });
    asm volatile("s_waitcnt vmcnt(%7)" : "+v"(r0), "+v"(r1), "+v"(c0), "+v"(c1), "+v"(c2), "+v"(c3), "+v"(bb) : "n"(NW8) : "memory");
    if (__builtin_amdgcn_ballot_w64(r0.y != c.tag || r1.y != c.tag || (!ISB && (c0.y != c.tag || c1.y != c.tag || c2.y != c.tag || c3.y != c.tag)))) {
#pragma unroll 1
      for (unsigned i = 0; i < SPINS; ++i) {
        stat();
        asm volatile("s_waitcnt vmcnt(0)" : "+v"(r0), "+v"(r1), "+v"(c0), "+v"(c1), "+v"(c2), "+v"(c3)::"memory");
        if (!__builtin_amdgcn_ballot_w64(r0.y != c.tag || r1.y != c.tag || (!ISB && (c0.y != c.tag || c1.y != c.tag || c2.y != c.tag || c3.y != c.tag)))) break;
      }
    }
    mb[0] = -lo(r0.x); rs[0] = __builtin_amdgcn_rcpf(hi(r0.x)); mb[1] = -lo(r1.x); rs[1] = __builtin_amdgcn_rcpf(hi(r1.x));
    asm volatile("" : "+v"(mb[0]), "+v"(mb[1]), "+v"(rs[0]), "+v"(rs[1]));
    if (!ISB) {
      *(LAS float*)(L + L_CS + tid * 4) = (__builtin_bit_cast(float, c0.x) + __builtin_bit_cast(float, c1.x)) + (__builtin_bit_cast(float, c2.x) + __builtin_bit_cast(float, c3.x));
      *(LAS float*)(L + L_B1 + tid * 4) = lo(bb);
    }
    base(0);
    close();
  }
  {
    auto h9 = [&](int n) {
      if (n < NW9) wl(NW8 + n);
      else if (!ISB && n < NW9 + 5) {
        const int j = n - NW9;
        xv[j] = *(const v2u*)((const char*)p.x + (size_t)(c.rt * 64 + w * 16 + tok) * (D * 2) + c.ct * 160 + j * 32 + g * 8);
        xv[5 + j] = *(const v2u*)((const char*)p.b2 + c.ct * 160 + j * 32 + g * 8);
      }
    };
    k1b(h9);
    k0(h9);
#pragma unroll
    for (int t = 1; t < NT; ++t) { mm(1, t, 0); mm(1, t, 1); tail(t - 1); }
    __builtin_amdgcn_s_setprio(ISB ? 1 : 3);
    asm volatile("s_nop 15" ::: "memory");
    SB();
  }
}
DI void hconst(Ctx& c, int pos, f32x4 (&k)[4]) {
  LAS char* t = c.L + pos * 2 + (c.lane >> 4) * 16;
  k[0] = *(LAS f32x4*)(t + L_CS); k[1] = *(LAS f32x4*)(t + L_CS + 64); k[2] = *(LAS f32x4*)(t + L_B1); k[3] = *(LAS f32x4*)(t + L_B1 + 64);
}
DI void hidden(Ctx& c, const f32x4& a00, const f32x4& a01, const f32x4& a10, const f32x4& a11, const f32x4 (&k)[4], unsigned dst, const float (&mb)[2], const float (&rs)[2]) {
#pragma unroll
  for (int a = 0; a < 2; ++a) {
    const float n = mb[a] * rs[a];
    f32x4 u = (a ? a10 : a00) * rs[a] + (k[0] * n + k[2]), v = (a ? a11 : a01) * rs[a] + (k[1] * n + k[3]);
#pragma unroll
    for (int i = 0; i < 4; ++i) { u[i] *= __builtin_fmaxf(u[i], 0.f); v[i] *= __builtin_fmaxf(v[i], 0.f); }
    st16(c.rw, dst + a * NKS * 1024, pk8(u, v));
  }
}

template <bool ISB>
DI void run(const P& p, const int w) {
  extern __shared__ __attribute__((aligned(16))) char smem[];
  LAS char* L = (LAS char*)smem;
  Ctx c;
  const int b = blockIdx.x, xcd = b & 7, r = b >> 3;
  if (K1MAP == 0) { c.panel = xcd * 2 + (r >> 4); c.cg = r & 15; }
  else if (K1MAP == 1) { c.panel = (xcd >> 1) * 4 + (r >> 3); c.cg = (r & 3) + (xcd & 1) * 4 + ((r >> 2) & 1) * 8; }
  else { c.panel = (xcd >> 2) * 8 + (r >> 2); c.cg = (r & 1) + (xcd & 3) * 2 + ((r >> 1) & 1) * 8; }
  if (K2MAP == 0) { c.rt = xcd * 4 + (r >> 3); c.ct = r & 7; }
  else { c.rt = (xcd >> 1) * 8 + (r >> 2); c.ct = (xcd & 1) * 4 + (r & 3); }
  const int tid = threadIdx.x, lane = tid & 63;
  const int g = lane >> 4, tok = lane & 15, panel = c.panel, cg = c.cg, pid = panel * 16 + cg;
  const bool KV = cg < 8;
  c.L = L; c.b = b; c.w = w; c.lane = lane;
  c.head = 4 * xcd + (r & 3); c.tb = r >> 2;
  c.rot = 0;
  c.rw = __builtin_amdgcn_make_buffer_rsrc(p.ws, 0, WS_END, 0x00020000);
  unsigned ep; v4u xa, xc;
  {
    const char* row = (const char*)p.x + (size_t)(panel * 128 + cg * 8 + w) * (D * 2);
    asm volatile("global_load_dword %0, %1, %2 sc1" : "=v"(ep) : "v"(0u), "s"(p.ws) : "memory");
    asm volatile("global_load_dwordx4 %0, %1, %2" : "=v"(xa) : "v"(lane * 16), "s"(row) : "memory");
    asm volatile("global_load_dwordx4 %0, %1, %2 offset:1024" : "=v"(xc) : "v"(tok * 16), "s"(row) : "memory");
  }
  const int pks0 = 3 * (b / 10) + w, pks = pks0 < NKS ? pks0 : NKS - 1, pcb = b % 10;
  {
    const char* src; unsigned o0, st;
    if (!ISB && w < 3) {
      if (pks < 54) { src = (const char*)p.w2 + (size_t)(32 * pks) * (D * 2); o0 = (lane >> 3) * (D * 2); st = 8 * D * 2; }
      else if (pks < 70) { src = (const char*)p.wo + (size_t)(40 * (pks - 54)) * (D * 2); o0 = ((lane >> 5) * 8 + ((lane >> 3) & 3)) * (D * 2); st = 20 * D * 2; }
      else { src = (const char*)p.wo + (size_t)(160 * (pks - 70) + 16) * (D * 2); o0 = ((lane >> 5) * 40 + ((lane >> 3) & 3)) * (D * 2); st = 80 * D * 2; }
      src += pcb * 128;
    } else {
      const int jb = b < 228 ? b : 0, lc = jb % 57, k0 = (jb / 57) * 160 + (w - 3) * 32;
      if (lc < 30) { src = (const char*)p.wqkv + (size_t)k0 * (3 * D * 2) + lc * 128; st = 8 * 3 * D * 2; }
      else { src = (const char*)p.w1 + (size_t)k0 * (F * 2) + (lc - 30) * 128; st = 8 * F * 2; }
      o0 = (lane >> 3) * (st >> 3);
    }
    o0 += (lane & 7) * 16;
    const unsigned st2 = (!ISB && w < 3 && pks >= 54) ? (pks < 70 ? 4 * D * 2 : 20 * D * 2) : 2 * st;
    const unsigned dst = 2 * BUF + w * 4096;
    gldso(src, o0, dst); gldso(src, o0 + st, dst + 1024); gldso(src, o0 + st2, dst + 2048); gldso(src, o0 + st2 + st, dst + 3072);
  }
  {
    const int col = img_col(cg, lane & 31), k = (lane >> 5) * 32 + (w & 3) * 8 + (w >> 2) * 4;
    const bool f1 = col >= 3 * D;
    const unsigned pitch = f1 ? F * 2 : 3 * D * 2;
    const char* q = (f1 ? (const char*)p.w1 - 3 * D * 2 : (const char*)p.wqkv) + (k * pitch + col * 2);
    c.wadv = 64 * pitch;
#pragma unroll
    for (int i = 0; i < 4; ++i) c.wp[i] = q + i * pitch - c.wadv;
    c.xb = (const char*)p.x + (size_t)(panel * 128 + w * 16) * (D * 2);
    c.xo0 = (lane >> 3) * (D * 2) + 16 * ((lane & 7) ^ (lane >> 4)); c.xo1 = (8 + (lane >> 3)) * (D * 2) + 16 * ((lane & 7) ^ (4 | (lane >> 4)));
  }
#pragma unroll
  for (int i = 0; i < 6; ++i) piece(c, i, w * 2048, XB + w * 4 * WR);
  if (lane < 25) gldso((const char*)p.wphi + (cg & 7) * 3200 + w * 400, lane * 16, L_WPHI + w * 400);
#pragma unroll
  for (int i = 0; i < 6; ++i) piece(c, i, BUF + w * 2048, BUF + XB + w * 4 * WR);
  if (ISB && w == 7) *(LAS v4u*)(L + L_ZERO + lane * 16) = v4u{0, 0, 0, 0};
  if (!ISB && tid < 16) *(LAS unsigned*)(L + L_CNT + tid * 4) = 0;
  asm volatile("s_waitcnt vmcnt(17)" : "+v"(ep), "+v"(xa), "+v"(xc)::"memory");
  c.tag = __builtin_amdgcn_readfirstlane(ep) + 1u;
  unsigned stat;
  {
    float v[16];
#pragma unroll
    for (int i = 0; i < 4; ++i) { v[2 * i] = lo(xa[i]); v[2 * i + 1] = hi(xa[i]); v[8 + 2 * i] = lo(xc[i]); v[9 + 2 * i] = hi(xc[i]); }
    float s0 = 0.f, s1 = 0.f;
#pragma unroll
    for (int i = 0; i < 8; ++i) { s0 += v[i]; s1 += v[8 + i]; }
    const float m = wave_sum(lane < 16 ? s0 + s1 : s0) * (1.f / D);
    s0 = 0.f; s1 = 0.f;
#pragma unroll
    for (int i = 0; i < 8; ++i) { s0 += (v[i] - m) * (v[i] - m); s1 += (v[8 + i] - m) * (v[8 + i] - m); }
    const float var = wave_sum(lane < 16 ? s0 + s1 : s0) * (1.f / D);
    stat = pk(m, __builtin_sqrtf(rb(rb(var) + 1e-5f)));
  }
  WAITV(13);
  v4u sf[4];
#pragma unroll
  for (int t = 0; t < 4; ++t) sf[t] = trd2(L + 2 * BUF + w * 4096 + (lane >> 2) * 128 + (lane & 3) * 8 + t * 32, 16 * 128);
  if (ISB || w == 3) {
    const v4u ones = {0x3f803f80u, 0x3f803f80u, 0x3f803f80u, 0x3f803f80u};
#pragma unroll
    for (int t = 0; t < 4; ++t) {
      f32x4 cs = mfma32(sf[t], ones, f32x4{0, 0, 0, 0});
      if (tok == 0) *(LAS f32x4*)(L + L_CP + (w - 3) * 256 + t * 64 + g * 16) = cs;
    }
  }
  WAITV(7);
  st8(c.rw, lane == 0 ? WS_RS + (panel * 128 + cg * 8 + w) * 8 : 0xfffffff0u, v2u{stat, c.tag});
  if (!ISB && w < 3) {
#pragma unroll
    for (int t = 0; t < 4; ++t) st16(c.rw, pks0 < NKS && b < 250 ? WS_WT + ((4 * pcb + t) * NKS + pks) * 1024 + lane * 16 : 0xffffffe0u, sf[t]);
  }
  WAITL0(); BAR(); SB();

  v4u wpre[6][5]; v2u xv[10];
  float mb[2], rs[2];
  const int dd = (cg & 1) * 32, rt = c.rt, ct = c.ct, aw = w & 3;
  const unsigned afr = WS_AFR + rt * 4 * NKS * 1024 + lane * 16, wt = WS_WT + ct * 5 * NKS * 1024 + lane * 16;
  const unsigned fh = sel(lane < 16, MB_H + (panel * 16 + lane) * 256 + cg * 4, OOB);
  const unsigned hd0 = WS_AFR + (panel * 8 + aw * 2) * NKS * 1024 + lane * 16;
  auto pack = [&](f32x4 (&v)[7], v4u (&bq)[3]) {
    bq[0] = pk8(v[0], v[1]); bq[1] = pk8(v[2], v[3]);
    const v2u e = pk4(v[4]); bq[2] = v4u{e.x, e.y, 0x00003f80u, 0u};
  };
  if (!ISB) {
    if (!KV) {
      f32x4 acc[2][8], hk[4][4];
      unsigned hd, hl;
      gemm1<0, 8, false>(p, c, 0, 256, acc, wpre, xv, mb, rs, [&](int n) {
        if (n < 4) hconst(c, 256 + 64 * n, hk[n]);
        if (n == 4) { hd = hd0 + (cg < 15 ? 5 * cg - 23 : 51) * 1024; hl = cg < 14 ? hd : OOB; asm volatile("" : "+v"(hd), "+v"(hl)); }
      });
#pragma unroll
      for (int u = 0; u < 4; ++u) hidden(c, acc[0][2 * u], acc[0][2 * u + 1], acc[1][2 * u], acc[1][2 * u + 1], hk[u], (u < 3 ? hd : hl) + u * 1024, mb, rs);
    } else {
      f32x4 acc[2][7], hk[4], cs[5];
      unsigned hd; LAS char* d;
      gemm1<5, 2, false>(p, c, 192 + dd, 384, acc, wpre, xv, mb, rs, [&](int n) {
        if (n == 0) hconst(c, 384, hk);
        if (n == 1) {
#pragma unroll
          for (int t = 0; t < 5; ++t) cs[t] = *(LAS f32x4*)(L + L_CS + (192 + dd) * 2 + g * 80 + t * 16);
        }
        if (n == 2) { hd = hd0 + 2 * cg * 1024; d = L + L_V + (w * 32 + tok) * PV + g * 48; asm volatile("" : "+v"(hd), "+v"(d)); }
      });
      hidden(c, acc[0][5], acc[0][6], acc[1][5], acc[1][6], hk, hd, mb, rs);
      SB();
#pragma unroll
      for (int a = 0; a < 2; ++a) {
        v4u bq[3];
        const float n = mb[a] * rs[a];
#pragma unroll
        for (int t = 0; t < 5; ++t) acc[a][t] = acc[a][t] * rs[a] + cs[t] * n;
        pack(acc[a], bq);
        *(LAS v4u*)(d + a * 16 * PV) = bq[0]; *(LAS v4u*)(d + a * 16 * PV + 16) = bq[1]; *(LAS v4u*)(d + a * 16 * PV + 32) = bq[2];
      }
      arrive(C_V);
    }
    constexpr int NK = 14;
    const unsigned ap = afr + (14 * w - (w == 3)) * 1024, bp = wt + (14 * w - (w == 3)) * 1024, apl = w < 2 ? ap : OOB, bpl = w < 2 ? bp : OOB;
    auto pld = [&](unsigned off, unsigned so) { SB(); asm volatile("s_waitcnt vmcnt(%0)" ::"n"(INF - 1)); const v4u v = ld16s(c.rw, off, so); SB(); return v; };
    const int nk = w < 2 ? 14 : 13;
    auto ko = [&](int k) { const int kk = c.rot + k; return (kk >= nk ? kk - nk : kk) * 1024; };
    auto wld = [&](int n) { return pld(n / 5 < NK - 1 ? bp : bpl, (n % 5) * NKS * 1024 + ko(n / 5)); };
    SB();
    __builtin_amdgcn_s_setprio(0);
#pragma unroll
    for (int n = 5; n < 8; ++n) wpre[n / 5][n % 5] = ld16s(c.rw, bp, (n % 5) * NKS * 1024 + ko(n / 5));
    SB();
    WAITV(3);
    arrive_flag(C_H, 7, c.rw, fh, c.tag);
#pragma unroll
    for (int n = 8; n < 30; ++n) wpre[n / 5][n % 5] = wld(n);
    LAS char* stg = L + w * NST * 1024 + lane * 16;
    f32x4 acc[4][5];
#pragma unroll
    for (int j = 0; j < 5; ++j) acc[3][j] = f32x4{lo(xv[j].x) + lo(xv[5 + j].x), hi(xv[j].x) + hi(xv[5 + j].x), lo(xv[j].y) + lo(xv[5 + j].y), hi(xv[j].y) + hi(xv[5 + j].y)};
    asm volatile("" : "+v"(acc[3][0]), "+v"(acc[3][1]), "+v"(acc[3][2]), "+v"(acc[3][3]), "+v"(acc[3][4]));
    await(L, C_H, 8);
    {
      v16u fv;
#pragma unroll
      for (int n = 0; n < NST; ++n) {
        if (n == NST - FLEAD) asm volatile("s_load_dwordx16 %0, %1, %2 glc" : "=s"(fv) : "s"(p.ws), "s"(MB_H + (rt * 8 + ct) * 256) : "memory");
        asm volatile("s_mov_b32 m0, %3\n\ts_waitcnt vmcnt(%4)\n\tbuffer_load_dwordx4 %0, %1, %2 offen sc1 lds" ::"v"(bp), "s"(c.rw), "s"(((30 + n) % 5) * NKS * 1024 + ko((30 + n) / 5)), "s"((w * NST + n) * 1024), "n"(INF - 1) : "memory");
      }
      asm volatile("s_waitcnt lgkmcnt(0)" : "+s"(fv)::"memory");
      if (differs<16>(fv, c.tag)) wait_flags<16>(p.ws, c.rw, MB_H + (rt * 8 + ct) * 256, 0, c.tag);
    }
    v4u ra[RA];
    unsigned so[4];
#pragma unroll
    for (int i = 0; i < 4; ++i) so[i] = ((w + 1 + i) & 3) * NKS * 1024;
    auto ald = [&](int m) { return pld(m / 4 < NK - 1 ? ap : apl, so[m % 4] + ko(m / 4)); };
#pragma unroll
    for (int m = 0; m < RA; ++m) ra[m] = ald(m);
#pragma unroll
    for (int k = 0; k < NK; ++k) {
#pragma unroll
      for (int i = 0; i < 4; ++i) {
#pragma unroll
        for (int j = 0; j < 5; ++j) {
          acc[i][j] = mfma32(wpre[k % 6][j], ra[(k * 4 + i) % RA], k || i == 3 ? acc[i][j] : f32x4{0, 0, 0, 0});
          const int n = i * 5 + j, f = (k + 5) * 5 + n / 4;
          if (k && k + 5 < NK && n % 4 == 1) {
            if (f < 30 + NST) { wpre[(k + 5) % 6][n / 4] = *(LAS v4u*)(stg + (f - 30) * 1024); SB(); }
            else wpre[(k + 5) % 6][n / 4] = wld(f);
          }
          if (k == NK - 1 && i) { *(LAS f32x4*)(stg + ((i - 1) * 5 + j) * 1024) = acc[i - 1][j]; SB(); }
        }
        if (k * 4 + i + RA < NK * 4) ra[(k * 4 + i) % RA] = ald(k * 4 + i + RA);
      }
      SB();
    }
    arrive(C_A);
    await(L, C_A, 4);
    LAS char* q0 = L + ((w + 3) & 3) * NST * 1024 + lane * 16;
    LAS char* q1 = L + ((w + 2) & 3) * NST * 1024 + 5120 + lane * 16;
    LAS char* q2 = L + ((w + 1) & 3) * NST * 1024 + 10240 + lane * 16;
#pragma unroll
    for (int j = 0; j < 5; ++j) acc[3][j] = (acc[3][j] + *(LAS f32x4*)(q0 + j * 1024)) + (*(LAS f32x4*)(q1 + j * 1024) + *(LAS f32x4*)(q2 + j * 1024));
#pragma unroll
    for (int j = 0; j < 5; ++j) *(LAS f32x4*)(q0 + j * 1024) = acc[3][j];
    asm volatile("hk_end0:");
    arrive(C_X + 4 * w);
  } else {
    const int head = c.head, tb = c.tb;
    {
      f32x4 acc[2][7], pq[2][5], hk[4], cs[5];
      unsigned hd; LAS char* wp;
      gemm1<5, 2, true>(p, c, dd, KV ? 448 : 192, acc, wpre, xv, mb, rs, [&](int n) {
        if (n == 0) hconst(c, KV ? 448 : 192, hk);
        if (n == 1) {
#pragma unroll
          for (int t = 0; t < 5; ++t) cs[t] = *(LAS f32x4*)(L + L_CS + dd * 2 + g * 80 + t * 16);
        }
        if (n == 2) {
          hd = hd0 + (KV ? 2 * cg + 1 : (cg < 15 ? 5 * cg - 24 : 50)) * 1024;
          wp = L + sel((lane & 3) == g, L_WPHI + g * 800 + ((lane & 15) >> 2) * 40, L_ZERO);
          asm volatile("" : "+v"(hd), "+v"(wp));
        }
      });
      {
        v4u a0[5], a1[5], bq[2][3]; v2u a2[5];
#pragma unroll
        for (int t = 0; t < 5; ++t) a0[t] = trd2(wp + t * 8, 160);
        hidden(c, acc[0][5], acc[0][6], acc[1][5], acc[1][6], hk, hd, mb, rs);
        SB();
#pragma unroll
        for (int t = 0; t < 5; ++t) { a1[t] = trd2(wp + 320 + t * 8, 160); a2[t] = trd(wp + 640 + t * 8); }
#pragma unroll
        for (int a = 0; a < 2; ++a) {
#pragma unroll
          for (int t = 0; t < 5; ++t) acc[a][t] += cs[t] * mb[a];
          pack(acc[a], bq[a]);
        }
        SB();
#pragma unroll
        for (int a = 0; a < 2; ++a) {
#pragma unroll
          for (int t = 0; t < 5; ++t) pq[a][t] = mfma32(a0[t], bq[a][0], f32x4{0, 0, 0, 0});
#pragma unroll
          for (int t = 0; t < 5; ++t) pq[a][t] = mfma32(a1[t], bq[a][1], pq[a][t]);
#pragma unroll
          for (int t = 0; t < 5; ++t) pq[a][t] = mfma32(v4u{a2[t].x, a2[t].y, 0u, 0u}, bq[a][2], pq[a][t]);
        }
      }
      SB();
      WAITV(0);
      arrive_flag(C_H, 7, c.rw, fh, c.tag);
#pragma unroll
      for (int a = 0; a < 2; ++a) {
        unsigned wv[20];
        dual_softmax(pq[a], rs[a], wv, [&]() {});
        const int tt = aw * 2 + a;
        if (KV) {
          LAS char* d = L + L_PHK + (tt * 16 + tok) * PK + g * 80;
          *(LAS v4u*)(d) = v4u{wv[0], wv[1], wv[4], wv[5]};
          *(LAS v4u*)(d + 16) = v4u{wv[8], wv[9], wv[12], wv[13]};
          *(LAS v4u*)(d + 32) = v4u{wv[2], wv[3], wv[6], wv[7]};
          *(LAS v4u*)(d + 48) = v4u{wv[10], wv[11], wv[14], wv[15]};
          *(LAS v4u*)(d + 64) = v4u{wv[16], wv[17], wv[18], wv[19]};
        } else {
          const unsigned d = WS_PHQ + ((panel * 8 + tt) * 32 + (cg - 8) * 4 + g) * 1280 + tok * 16;
#pragma unroll
          for (int i = 0; i < 5; ++i) st16(c.rw, d + i * 256, v4u{wv[4 * i], wv[4 * i + 1], wv[4 * i + 2], wv[4 * i + 3]});
        }
        SB();
      }
    }
    if (KV) {
      arrive(C_V);
      await(L, C_V, 8);
      f32x4 kv[2][3];
      LAS char* A = L + L_PHK + (lane >> 2) * PK + aw * 80 + (lane & 3) * 8;
      LAS char* Bv = L + L_V + (lane >> 2) * PV + aw * 48 + (lane & 3) * 16;
#pragma unroll
      for (int s = 0; s < 4; ++s) {
        const v4u b0 = trd2(Bv + s * 32 * PV, 16 * PV), b1 = trd2(Bv + 8 + s * 32 * PV, 16 * PV);
#pragma unroll
        for (int T = 0; T < 3; ++T) {
          const v4u f = trd2(A + s * 32 * PK + T * 32, 16 * PK);
          kv[0][T] = mfma32(f, b0, s ? kv[0][T] : f32x4{0, 0, 0, 0}); kv[1][T] = mfma32(f, b1, s ? kv[1][T] : f32x4{0, 0, 0, 0});
        }
      }
      const unsigned d = WS_KVP + ((cg * 4 + aw) * 16 + panel) * KVB;
      const unsigned d0 = tok < 12 ? d + (g * 12 + tok) * 16 : OOB, d1 = tok < 9 ? d + (g * 9 + tok) * 16 : OOB;
#pragma unroll
      for (int T = 0; T < 3; ++T) {
        st16(c.rw, (T < 2 || lane < 32 ? d0 : OOB) + T * 1344, __builtin_bit_cast(v4u, kv[0][T]));
        st16(c.rw, (T < 2 || lane < 32 ? d1 : OOB) + T * 1344 + (T < 2 ? 768 : 384), __builtin_bit_cast(v4u, kv[1][T]));
      }
      SB();
      st4(c.rw, sel(lane < 8, MB_K + ((cg * 4 + aw) * 8 + lane) * 128 + panel * 4, OOB), c.tag);
    }
#pragma unroll
    for (int j = 0; j < 5; ++j) wpre[4][j] = ld16(c.rw, wt + (j * NKS + 70 + aw) * 1024);
    SB();
    if (!KV) {
      WAITV(5);
      arrive_flag(C_Q, 3, c.rw, sel(lane < 4, MB_Q + (((cg - 8) * 4 + lane) * 8 + (panel >> 1)) * 128 + (panel & 1) * 4, OOB), c.tag);
    }
    v16u q0; v8u q1;
    {
      const int ch = aw * 56 + lane, T = (ch >= 84) + (ch >= 168), r0 = ch - 84 * T, n0 = T < 2 ? 48 : 24, dt = r0 >= n0, r1 = r0 - dt * n0;
      const int kg = (r1 * (dt ? 57 : 43)) >> 9, kc = r1 - kg * (dt ? 9 : 12);
      const bool on = lane < 56 && ch < 210;
      const unsigned ko = on ? WS_KVP + head * 16 * KVB + ch * 16 : OOB;
      const unsigned qo = WS_PHQ + ((tb * 16 + aw * 4) * 32 + head) * 1280, qo1 = lane < 32 ? qo + 1024 + tok * 16 + g * 8 : OOB;
      int kw = L_KV + sel(!on, 2560 + lane * 8, sel(T < 2, ((dt * 4 + kg) * 16 + kc) * 16 + T * 8, 2048 + ((dt * 2 + kg) * 16 + kc) * 8));
      asm volatile("" : "+v"(kw));
      f32x32 ka, kb; unsigned t0, t1, st, it; wait_flags<17>(p.ws, c.rw, MB_K + (head * 8 + tb) * 128, MB_Q + (head * 8 + tb) * 128 + (aw >> 1) * 4, c.tag);
      asm volatile(
        "s_mov_b32 %[st], 0\n\t" "buffer_load_dwordx4 v[192:195], %[ko], %[rw], %[st] offen sc1\n\t" "buffer_load_dwordx4 v[196:199], %[ko], %[rw], %[st] offen offset:3456 sc1\n\t"
        "s_mov_b32 %[st], 6912\n\t" "buffer_load_dwordx4 v[200:203], %[ko], %[rw], %[st] offen sc1\n\t" "buffer_load_dwordx4 v[204:207], %[ko], %[rw], %[st] offen offset:3456 sc1\n\t"
        "s_mov_b32 %[st], 13824\n\t" "buffer_load_dwordx4 v[208:211], %[ko], %[rw], %[st] offen sc1\n\t" "buffer_load_dwordx4 v[212:215], %[ko], %[rw], %[st] offen offset:3456 sc1\n\t"
        "s_mov_b32 %[st], 20736\n\t" "buffer_load_dwordx4 v[216:219], %[ko], %[rw], %[st] offen sc1\n\t" "buffer_load_dwordx4 v[220:223], %[ko], %[rw], %[st] offen offset:3456 sc1\n\t"
        "s_mov_b32 %[st], 27648\n\t" "buffer_load_dwordx4 v[224:227], %[ko], %[rw], %[st] offen sc1\n\t" "buffer_load_dwordx4 v[228:231], %[ko], %[rw], %[st] offen offset:3456 sc1\n\t"
        "s_mov_b32 %[st], 34560\n\t" "buffer_load_dwordx4 v[232:235], %[ko], %[rw], %[st] offen sc1\n\t" "buffer_load_dwordx4 v[236:239], %[ko], %[rw], %[st] offen offset:3456 sc1\n\t"
        "s_mov_b32 %[st], 41472\n\t" "buffer_load_dwordx4 v[240:243], %[ko], %[rw], %[st] offen sc1\n\t" "buffer_load_dwordx4 v[244:247], %[ko], %[rw], %[st] offen offset:3456 sc1\n\t"
        "s_mov_b32 %[st], 48384\n\t" "buffer_load_dwordx4 v[248:251], %[ko], %[rw], %[st] offen sc1\n\t" "buffer_load_dwordx4 v[252:255], %[ko], %[rw], %[st] offen offset:3456 sc1\n\t"
        "s_mov_b32 %[st], 0\n\t" "buffer_load_dwordx4 v[168:171], %[qa], %[rw], %[st] offen sc1\n\t" "buffer_load_dwordx2 v[184:185], %[qb], %[rw], %[st] offen sc1\n\t"
        "s_mov_b32 %[st], 40960\n\t" "buffer_load_dwordx4 v[172:175], %[qa], %[rw], %[st] offen sc1\n\t" "buffer_load_dwordx2 v[186:187], %[qb], %[rw], %[st] offen sc1\n\t"
        "s_mov_b32 %[st], 81920\n\t" "buffer_load_dwordx4 v[176:179], %[qa], %[rw], %[st] offen sc1\n\t" "buffer_load_dwordx2 v[188:189], %[qb], %[rw], %[st] offen sc1\n\t"
        "s_mov_b32 %[st], 122880\n\t" "buffer_load_dwordx4 v[180:183], %[qa], %[rw], %[st] offen sc1\n\t" "buffer_load_dwordx2 v[190:191], %[qb], %[rw], %[st] offen sc1\n\t"
        "s_mov_b32 %[it], %[sp]\n\t" "s_waitcnt vmcnt(8)\n\t" "1:\tv_max3_u32 %[t0], v192, v193, v194\n\t" "v_max3_u32 %[t1], v195, v196, v197\n\t" "v_max3_u32 %[t0], %[t0], v198, v199\n\t"
        "v_max3_u32 %[t1], %[t1], v200, v201\n\t" "v_max3_u32 %[t0], %[t0], v202, v203\n\t" "v_max3_u32 %[t1], %[t1], v204, v205\n\t" "v_max3_u32 %[t0], %[t0], v206, v207\n\t"
        "v_max3_u32 %[t1], %[t1], v208, v209\n\t" "v_max3_u32 %[t0], %[t0], v210, v211\n\t" "v_max3_u32 %[t1], %[t1], v212, v213\n\t" "v_max3_u32 %[t0], %[t0], v214, v215\n\t"
        "v_max3_u32 %[t1], %[t1], v216, v217\n\t" "v_max3_u32 %[t0], %[t0], v218, v219\n\t" "v_max3_u32 %[t1], %[t1], v220, v221\n\t" "v_max3_u32 %[t0], %[t0], v222, v223\n\t"
        "v_max3_u32 %[t1], %[t1], v224, v225\n\t" "v_max3_u32 %[t0], %[t0], v226, v227\n\t" "v_max3_u32 %[t1], %[t1], v228, v229\n\t" "v_max3_u32 %[t0], %[t0], v230, v231\n\t"
        "v_max3_u32 %[t1], %[t1], v232, v233\n\t" "v_max3_u32 %[t0], %[t0], v234, v235\n\t" "v_max3_u32 %[t1], %[t1], v236, v237\n\t" "v_max3_u32 %[t0], %[t0], v238, v239\n\t"
        "v_max3_u32 %[t1], %[t1], v240, v241\n\t" "v_max3_u32 %[t0], %[t0], v242, v243\n\t" "v_max3_u32 %[t1], %[t1], v244, v245\n\t" "v_max3_u32 %[t0], %[t0], v246, v247\n\t"
        "v_max3_u32 %[t1], %[t1], v248, v249\n\t" "v_max3_u32 %[t0], %[t0], v250, v251\n\t" "v_max3_u32 %[t1], %[t1], v252, v253\n\t" "v_max3_u32 %[t0], %[t0], v254, v255\n\t"
        "v_max_u32 %[t0], %[t0], %[t1]\n\t" "v_cmp_eq_u32 vcc, -1, %[t0]\n\t" "s_cbranch_vccz 2f\n\t" "s_mov_b32 %[st], 0\n\t" "buffer_load_dwordx4 v[192:195], %[ko], %[rw], %[st] offen sc1\n\t"
        "buffer_load_dwordx4 v[196:199], %[ko], %[rw], %[st] offen offset:3456 sc1\n\t" "s_mov_b32 %[st], 6912\n\t" "buffer_load_dwordx4 v[200:203], %[ko], %[rw], %[st] offen sc1\n\t"
        "buffer_load_dwordx4 v[204:207], %[ko], %[rw], %[st] offen offset:3456 sc1\n\t" "s_mov_b32 %[st], 13824\n\t" "buffer_load_dwordx4 v[208:211], %[ko], %[rw], %[st] offen sc1\n\t"
        "buffer_load_dwordx4 v[212:215], %[ko], %[rw], %[st] offen offset:3456 sc1\n\t" "s_mov_b32 %[st], 20736\n\t" "buffer_load_dwordx4 v[216:219], %[ko], %[rw], %[st] offen sc1\n\t"
        "buffer_load_dwordx4 v[220:223], %[ko], %[rw], %[st] offen offset:3456 sc1\n\t" "s_mov_b32 %[st], 27648\n\t" "buffer_load_dwordx4 v[224:227], %[ko], %[rw], %[st] offen sc1\n\t"
        "buffer_load_dwordx4 v[228:231], %[ko], %[rw], %[st] offen offset:3456 sc1\n\t" "s_mov_b32 %[st], 34560\n\t" "buffer_load_dwordx4 v[232:235], %[ko], %[rw], %[st] offen sc1\n\t"
        "buffer_load_dwordx4 v[236:239], %[ko], %[rw], %[st] offen offset:3456 sc1\n\t" "s_mov_b32 %[st], 41472\n\t" "buffer_load_dwordx4 v[240:243], %[ko], %[rw], %[st] offen sc1\n\t"
        "buffer_load_dwordx4 v[244:247], %[ko], %[rw], %[st] offen offset:3456 sc1\n\t" "s_mov_b32 %[st], 48384\n\t" "buffer_load_dwordx4 v[248:251], %[ko], %[rw], %[st] offen sc1\n\t"
        "buffer_load_dwordx4 v[252:255], %[ko], %[rw], %[st] offen offset:3456 sc1\n\t" "s_waitcnt vmcnt(0)\n\t" "s_sub_u32 %[it], %[it], 1\n\t" "s_cbranch_scc0 1b\n\t" "2:"
        : "=&{v[192:223]}"(ka), "=&{v[224:255]}"(kb), "=&{v[168:183]}"(q0), "=&{v[184:191]}"(q1), [t0] "=&v"(t0), [t1] "=&v"(t1), [st] "=&s"(st), [it] "=&s"(it)
        : [ko] "v"(ko), [qa] "v"(qo + lane * 16), [qb] "v"(qo1), [rw] "s"(c.rw), [sp] "n"(SPINS) : "memory", "vcc", "scc");
      ka += kb;
      const f32x16 kc16 = ka.lo + ka.hi;
      const f32x8 kc8 = kc16.lo + kc16.hi;
      *(LAS v2u*)(L + kw) = pk4(kc8.lo + kc8.hi);
    }
    arrive(C_K);
    await(L, C_K, 4);
    {
      const int c0 = tok < 12 ? tok : 0, c1 = tok < 8 ? tok : 8;
      const v4u A00 = *(LAS v4u*)(L + L_KV + (g * 16 + c0) * 16), A01 = *(LAS v4u*)(L + L_KV + 1024 + (g * 16 + c1) * 16);
      const v2u A10 = *(LAS v2u*)(L + L_KV + 2048 + ((g & 1) * 16 + c0) * 8), A11 = *(LAS v2u*)(L + L_KV + 2304 + ((g & 1) * 16 + c1) * 8);
      asm volatile("s_waitcnt vmcnt(0)" : "+{v[168:183]}"(q0), "+{v[184:191]}"(q1)::"memory");
      const unsigned ob = WS_AFR + (tb * 16 + aw * 4) * NKS * 1024;
      const unsigned oa = sel(lane < 32, ob + (54 + (head >> 1)) * 1024 + (head & 1) * 512 + lane * 16, OOB), oc = sel(g == 2, ob + (70 + (head >> 3)) * 1024 + (head & 7) * 128 + tok * 8, OOB);
      f32x4 o0[4], o1[4];
#pragma unroll
      for (int i = 0; i < 4; ++i) { o1[i] = mfma32(A01, sub4(q0, i), f32x4{0, 0, 0, 0}); o0[i] = mfma32(A00, sub4(q0, i), f32x4{0, 0, 0, 0}); }
#pragma unroll
      for (int i = 0; i < 4; ++i) { const v2u e = {q1[2 * i], q1[2 * i + 1]}; o1[i] = mfma16(A11, e, o1[i]); o0[i] = mfma16(A10, e, o0[i]); }
#pragma unroll
      for (int i = 0; i < 4; ++i) {
        float u = o1[i][0] + 1e-6f, den = u;
        asm volatile("s_nop 1\n\tv_permlane32_swap_b32 %0, %1" : "+v"(u), "+v"(den));
        const float rd = __builtin_amdgcn_rcpf(den);
        const v4u v = pk8(o0[i] * rd, o1[i] * rd);
        st16(c.rw, oa + i * NKS * 1024, v);
        st8(c.rw, oc + i * NKS * 1024, v2u{v.x, v.y});
      }
      SB();
      st4(c.rw, sel(lane < 8, MB_O + ((tb * 4 + aw) * 8 + lane) * 256 + head * 4, OOB), c.tag);
    }
    {
      v16u of[4]; v4u og[4]; unsigned t0, t1, it;
      const unsigned fo = afr + (54 + 4 * aw) * 1024, ro = WS_AFR + (rt * 4 * NKS + 70 + aw) * 1024 + g * 256 + tok * 8;
      unsigned so[4];
#pragma unroll
      for (int n = 0; n < 4; ++n) so[n] = ((aw + 1 + n) & 3) * NKS * 1024;
      wait_flags<8>(p.ws, c.rw, MB_O + (rt * 8 + ct) * 256 + aw * 32, 0, c.tag);
      __builtin_amdgcn_s_waitcnt(0x0f70);
      SB();
      O_ISSUE0(); O_ISSUE1(); O_ISSUE2(); O_ISSUE3();
      f32x4 acc[2][5], t[5];
      unsigned cb, cx;
#pragma unroll
      for (int n = 0; n < 4; ++n) {
        if (n == 0) O_GET0(); else if (n == 1) O_GET1(); else if (n == 2) O_GET2(); else if (n == 3) O_GET3();
#pragma unroll
        for (int k = 0; k < 5; ++k) {
#pragma unroll
          for (int j = 0; j < 5; ++j) {
            acc[n & 1][j] = mfma32(wpre[k][j], k < 4 ? sub4(of[n], k) : og[n], k ? acc[n & 1][j] : f32x4{0, 0, 0, 0});
            if (n && k == 0) { *(LAS f32x4*)(L + L_XB + (((aw + n) & 3) * 3 + n - 1) * 5120 + j * 1024 + lane * 16) = acc[~n & 1][j]; SB(); }
          }
          if (n == 3 && k == 0) arrive(C_B);
          if (n == 3 && k == 1) { cb = peekv(L, C_B); cx = peekv(L, C_X + 4 * aw); }
          if (n == 3 && k == 2) {
            if (__builtin_amdgcn_readfirstlane(cb) < 4) await(L, C_B, 4);
            if (__builtin_amdgcn_readfirstlane(cx) < 1) await(L, C_X + 4 * aw, 1);
            asm volatile("" ::: "memory");
#pragma unroll
            for (int j = 0; j < 5; ++j) {
              LAS char* q = L + L_XB + aw * 15360 + j * 1024 + lane * 16;
              t[j] = (*(LAS f32x4*)q + *(LAS f32x4*)(q + 5120)) + (*(LAS f32x4*)(q + 10240) + *(LAS f32x4*)(L + ((aw + 3) & 3) * NST * 1024 + j * 1024 + lane * 16));
            }
          } else if (n < 3 || k < 2) SB();
        }
      }
      asm volatile("hk_end1:");
#pragma unroll
      for (int j = 0; j < 5; ++j) *(v2u*)((char*)p.out + (size_t)(rt * 64 + aw * 16 + tok) * (D * 2) + ct * 160 + j * 32 + g * 8) = pk4(acc[1][j] + t[j]);
    }
  }
}
__global__ __launch_bounds__(512, 2) void fk(P p) {
  const int w = __builtin_amdgcn_readfirstlane(threadIdx.x >> 6);
  {
    u64 pc; unsigned n;
    asm volatile("s_getpc_b64 %0\nhk_beg:\ts_mov_b32 %1, hk_end0-hk_beg\n\ts_max_i32 %1, %1, hk_end1-hk_beg" : "=s"(pc), "=s"(n)::"scc");
    asm volatile("s_mov_b32 m0, %2\n\ts_nop 0\n\tglobal_load_lds_dword %0, %1" ::"v"(__builtin_elementwise_min((((blockIdx.x >> 3) * 8 + w) * 2 + (threadIdx.x & 1)) * 128, n)), "s"(pc), "n"(L_KV) : "memory");
  }
  if (w < 4) run<false>(p, w); else run<true>(p, w);
}
}

extern "C" void kernel_launch(void* const* d_in, const int* in_sizes, int n_in, void* d_out, int out_size, void* d_ws, size_t ws_size, hipStream_t stream) {
  using namespace hk;
  P p;
  p.x = (const u16*)d_in[0]; p.wqkv = (const u16*)d_in[1]; p.wphi = (const u16*)d_in[2]; p.wo = (const u16*)d_in[3];
  p.w1 = (const u16*)d_in[5]; p.b1 = (const u16*)d_in[6]; p.w2 = (const u16*)d_in[7]; p.b2 = (const u16*)d_in[8];
  p.out = (u16*)d_out; p.ws = (char*)d_ws;
  (void)hipFuncSetAttribute((const void*)fk, hipFuncAttributeMaxDynamicSharedMemorySize, L_BYTES);
  fk<<<256, 512, L_BYTES, stream>>>(p);
}
```

```cpp
#include <hip/hip_runtime.h>
#include <hip/hip_bf16.h>
#include <stdint.h>

#define GAS __attribute__((address_space(1)))
#define LAS __attribute__((address_space(3)))
#define DI __device__ __forceinline__
typedef unsigned short u16;
typedef unsigned long long u64;
typedef unsigned v4u __attribute__((ext_vector_type(4)));
typedef unsigned v2u __attribute__((ext_vector_type(2)));
typedef float f32x4 __attribute__((ext_vector_type(4)));
typedef unsigned v8u __attribute__((ext_vector_type(8)));
typedef unsigned v16u __attribute__((ext_vector_type(16)));
typedef float f32x8 __attribute__((ext_vector_type(8)));
typedef float f32x16 __attribute__((ext_vector_type(16)));
typedef float f32x32 __attribute__((ext_vector_type(32)));
typedef float f32x2 __attribute__((ext_vector_type(2)));
typedef short s16x4 __attribute__((ext_vector_type(4)));
typedef short s16x8 __attribute__((ext_vector_type(8)));
typedef __bf16 bf16x2 __attribute__((ext_vector_type(2)));

namespace hk {
constexpr int S = 2048, D = 640, F = 1728, NKS = 74;
constexpr int NCOL = 3 * D + F;
constexpr unsigned WS_EPOCH = 0;
constexpr unsigned WS_FLAG = 4096;
constexpr unsigned FL_PT = WS_FLAG;
constexpr unsigned MB_H = WS_FLAG + 4096, MB_X = MB_H + 64, MB_O = MB_H + 128;
constexpr unsigned MB_K = MB_H + 65536, MB_Q = MB_K + 64;
constexpr unsigned WS_RS = MB_K + 32768;
constexpr unsigned WS_CS = WS_RS + S * 4;
constexpr unsigned WS_AFR = 262144;
constexpr unsigned WS_WT = WS_AFR + 128 * NKS * 1024;
constexpr unsigned WS_PHQ = WS_WT + 40 * NKS * 1024;
constexpr unsigned KVB = 1792;
constexpr unsigned WS_KVP = WS_PHQ + 32 * 32 * 3072;
constexpr unsigned WS_XCH = WS_KVP + 32 * 16 * KVB;
constexpr unsigned WS_END = WS_XCH + 256 * 20480;
static_assert(WS_CS + 4 * NCOL * 4 <= WS_AFR, "ws");
constexpr int XB = 16384, WR = 1056, BUF = XB + 32 * WR;
constexpr int L_WPHI = 3 * BUF, L_ZERO = L_WPHI + 3328, L_CS = L_ZERO + 1024, L_B1 = L_CS + 1024, L_CP = L_B1 + 1024, L_RS = L_CP, L_ST = L_CP + 1280, L_CNT = L_ST + 32, L_KV = L_CNT + 64, L_BYTES = L_KV + 3072 + 1024;
constexpr int PK = 352, PV = 224;
constexpr int L_V = 3 * BUF - 128 * PV - 64, L_PHK = L_V - 128 * PK - 64;
constexpr int AW = 15360, L_RES = 4 * AW, L_XB = L_PHK;
constexpr int C_H = L_CNT, C_K = L_CNT + 4, C_V = L_CNT + 8, C_Q = L_CNT + 12, C_B = L_CNT + 20, C_X = L_CNT + 24, C_A = L_CNT + 40;
constexpr unsigned OOB = 0xf0000000u;
static_assert(L_PHK >= BUF && L_RES >= BUF && L_RES + 12288 <= L_PHK && L_XB + 61440 <= 3 * BUF && L_BYTES <= 163840, "lds");

DI f32x4 mfma32(v4u a, v4u b, f32x4 c) { return __builtin_amdgcn_mfma_f32_16x16x32_bf16(__builtin_bit_cast(s16x8, a), __builtin_bit_cast(s16x8, b), c, 0, 0, 0); }
DI f32x4 mfma16(v2u a, v2u b, f32x4 c) { return mfma32(v4u{a.x, a.y, 0u, 0u}, v4u{b.x, b.y, 0u, 0u}, c); }
DI v2u trd(LAS char* p) { return __builtin_bit_cast(v2u, __builtin_amdgcn_ds_read_tr16_b64_v4i16((LAS s16x4*)p)); }
DI v4u trd2(LAS char* p, int d) { v2u a = trd(p), b = trd(p + d); return v4u{a.x, a.y, b.x, b.y}; }
DI unsigned pk(float a, float b) { f32x2 v = {a, b}; return __builtin_bit_cast(unsigned, __builtin_convertvector(v, bf16x2)); }
DI v2u pk4(f32x4 a) { return v2u{pk(a[0], a[1]), pk(a[2], a[3])}; }
DI v4u pk8(f32x4 a, f32x4 b) { return v4u{pk(a[0], a[1]), pk(a[2], a[3]), pk(b[0], b[1]), pk(b[2], b[3])}; }
DI float lo(unsigned u) { return __builtin_bit_cast(float, u << 16); }
DI float hi(unsigned u) { return __builtin_bit_cast(float, u & 0xffff0000u); }
DI float rb(float a) { return lo(pk(a, a)); }
DI void glds(const void* g, unsigned dst) { asm volatile("s_mov_b32 m0, %1\n\ts_nop 0\n\tglobal_load_lds_dwordx4 %0, off" ::"v"(g), "s"(dst) : "memory"); }
DI void gldso(const void* base, unsigned off, unsigned dst) { asm volatile("s_mov_b32 m0, %2\n\ts_nop 0\n\tglobal_load_lds_dwordx4 %0, %1" ::"v"(off), "s"(base), "s"(dst) : "memory"); }
DI float dot2(unsigned a, unsigned b, float c) { return __builtin_amdgcn_fdot2_f32_bf16(__builtin_bit_cast(bf16x2, a), __builtin_bit_cast(bf16x2, b), c, false); }
DI int lane_id() { int l; asm volatile("v_mbcnt_lo_u32_b32 %0, -1, 0\n\tv_mbcnt_hi_u32_b32 %0, -1, %0" : "=v"(l)); return l; }
#define BAR() __builtin_amdgcn_s_barrier()
#define WAITV(n) asm volatile("s_waitcnt vmcnt(" #n ")" ::: "memory")
#define WAITL0() asm volatile("s_waitcnt lgkmcnt(0)" ::: "memory")
#define SB() __builtin_amdgcn_sched_barrier(0)
typedef __amdgpu_buffer_rsrc_t rsrc_t;
DI v4u ld16(rsrc_t r, unsigned off) { return __builtin_amdgcn_raw_buffer_load_b128(r, off, 0, 16); }
DI v4u ld16s(rsrc_t r, unsigned off, unsigned so) { return __builtin_amdgcn_raw_buffer_load_b128(r, off, so, 16); }
DI v2u ld8(rsrc_t r, unsigned off) { return __builtin_amdgcn_raw_buffer_load_b64(r, off, 0, 16); }
DI unsigned ld4(rsrc_t r, unsigned off) { return __builtin_amdgcn_raw_buffer_load_b32(r, off, 0, 16); }
DI void st16(rsrc_t r, unsigned off, v4u v) { __builtin_amdgcn_raw_buffer_store_b128(v, r, off, 0, 16); }
DI void st8(rsrc_t r, unsigned off, v2u v) { __builtin_amdgcn_raw_buffer_store_b64(v, r, off, 0, 16); }
DI void st4(rsrc_t r, unsigned off, unsigned v) { __builtin_amdgcn_raw_buffer_store_b32(v, r, off, 0, 16); }
constexpr unsigned SPINS = 1u << 16;
#define INF 12
#define FLEAD 8
#define NWT 250
#define LDJ 1
#define XD 8
#define HPAR 1
#define NP1 6
#define NP0 0
#define STL 1
#define CWV 0
#define ROT 0, 1, 2, 3
#define PUBS 0
#define SENS 0
#define FLS ((PUBS > SENS ? PUBS : SENS) + 1)
#define QH 0
#define QE 0
#define PB0 2
#define PA0 1
#define PB1 0
#define BW 1
#define RA 8
#define K1MAP 1
#define K2MAP 1
DI void wait_tail(rsrc_t r, unsigned off, unsigned tag) {
#pragma unroll 1
  for (unsigned i = 0; i < SPINS; ++i) {
    asm volatile("" ::: "memory");
    if (__builtin_amdgcn_ballot_w64(ld4(r, off) != tag) == 0) break;
  }
}
template <int N, class V> DI unsigned differs(const V& v, unsigned tag) {
  u64 a = ~0ull, o = 0;
#pragma unroll
  for (int i = 0; i < N; i += 2) { const u64 x = (u64)v[i + 1] << 32 | v[i]; a &= x; o |= x; }
  unsigned l = (unsigned)a & (unsigned)(a >> 32), h = (unsigned)o | (unsigned)(o >> 32);
  asm("" : "+s"(l), "+s"(h));
  return (l ^ tag) | (h ^ tag);
}
template <int N> DI void wait_flags(const char* ws, rsrc_t r, unsigned off, unsigned off1, unsigned tag) {
  unsigned d = 1;
#pragma unroll 1
  for (unsigned i = 0; i < 4096 && d; ++i) {
    if (N == 1) { unsigned v; asm volatile("s_load_dword %0, %1, %2 glc\n\ts_waitcnt lgkmcnt(0)" : "=s"(v) : "s"(ws), "s"(off) : "memory"); d = v ^ tag; }
    else if (N == 8) { v8u v; asm volatile("s_load_dwordx8 %0, %1, %2 glc\n\ts_waitcnt lgkmcnt(0)" : "=s"(v) : "s"(ws), "s"(off) : "memory"); d = differs<8>(v, tag); }
    else if (N == 16) { v16u v; asm volatile("s_load_dwordx16 %0, %1, %2 glc\n\ts_waitcnt lgkmcnt(0)" : "=s"(v) : "s"(ws), "s"(off) : "memory"); d = differs<16>(v, tag); }
    else { v16u v; unsigned u; asm volatile("s_load_dwordx16 %0, %2, %3 glc\n\ts_load_dword %1, %2, %4 glc\n\ts_waitcnt lgkmcnt(0)" : "=&s"(v), "=&s"(u) : "s"(ws), "s"(off), "s"(off1) : "memory"); d = differs<16>(v, tag) | (u ^ tag); }
  }
  if (d) { const unsigned l = threadIdx.x & 63; wait_tail(r, N == 17 && l == 16 ? off1 : off + (l & (N == 17 ? 15 : N - 1)) * 4, tag); }
}
DI void arrive(int c) { asm volatile("s_mov_b64 exec, 1\n\tds_add_u32 %0, %1\n\ts_mov_b64 exec, -1" ::"v"(c), "v"(1u) : "memory"); }
DI void arrive_flag(int c, unsigned last, rsrc_t r, unsigned off, unsigned tag) {
  unsigned t, u;
  asm volatile("s_mov_b64 exec, 1\n\tds_add_rtn_u32 %0, %2, %3\n\ts_mov_b64 exec, -1\n\ts_waitcnt lgkmcnt(0)\n\tv_readfirstlane_b32 %1, %0\n\ts_cmp_lg_u32 %1, %4\n\ts_cbranch_scc1 1f\n\tbuffer_store_dword %5, %6, %7, 0 offen sc1\n1:"
               : "=&v"(t), "=&s"(u) : "v"(c), "v"(1u), "n"(last), "v"(tag), "v"(off), "s"(r) : "memory", "scc");
}
DI unsigned peekv(LAS char* L, int c) { return __hip_atomic_load((LAS unsigned*)(L + c), __ATOMIC_RELAXED, __HIP_MEMORY_SCOPE_WORKGROUP); }
DI unsigned peek(LAS char* L, int c) { return __builtin_amdgcn_readfirstlane(peekv(L, c)); }
DI void await(LAS char* L, int c, unsigned n) {
  #pragma unroll 1
  for (unsigned i = 0; i < (1u << 22) && peek(L, c) < n; ++i) __builtin_amdgcn_s_sleep(1);
  asm volatile("" ::: "memory");
}
DI void recheck(unsigned v, int c, unsigned n) {
  unsigned t, i;
  asm volatile("s_mov_b32 %2, 0x400000\n1:\tv_readfirstlane_b32 %1, %0\n\ts_cmp_ge_u32 %1, %4\n\ts_cbranch_scc1 2f\n\ts_sleep 1\n\tv_mov_b32 %0, %3\n\tds_read_b32 %0, %0\n\ts_sub_u32 %2, %2, 1\n\ts_waitcnt lgkmcnt(0)\n\ts_cbranch_scc0 1b\n2:"
               : "+v"(v), "=&s"(t), "=&s"(i) : "n"(c), "n"(n) : "memory", "scc");
}
DI unsigned sel(bool c, unsigned a, unsigned b) { return c ? a : b; }
DI v4u sub4(const v16u& v, int i) { return v4u{v[4 * i], v[4 * i + 1], v[4 * i + 2], v[4 * i + 3]}; }
#define K_ISSUE() asm volatile( \
        "buffer_load_dwordx4 v[224:227], %[ko], %[rw], 0 offen sc1\n\t" "buffer_load_dwordx4 v[228:231], %[ko], %[rw], 0 offen offset:1792 sc1\n\t"  \
        "buffer_load_dwordx4 v[232:235], %[ko], %[rw], 0 offen offset:3584 sc1\n\t" "buffer_load_dwordx4 v[236:239], %[ko], %[rw], %[s1] offen sc1\n\t"  \
        "buffer_load_dwordx4 v[240:243], %[ko], %[rw], %[s1] offen offset:1792 sc1\n\t" "buffer_load_dwordx4 v[244:247], %[ko], %[rw], %[s1] offen offset:3584 sc1\n\t"  \
        "buffer_load_dwordx4 v[248:251], %[ko], %[rw], %[s2] offen sc1\n\t" "buffer_load_dwordx4 v[252:255], %[ko], %[rw], %[s2] offen offset:1792 sc1\n\t" "" \
        : "+{v[224:239]}"(ka), "+{v[240:255]}"(kb) : [ko] "v"(ko), [rw] "s"(c.rw), [s1] "s"(3 * KVB), [s2] "s"(6 * KVB) : "memory")
#define K_GET() asm volatile( \
        "s_mov_b32 %[it], %[sp]\n\t" "s_waitcnt vmcnt(0)\n\t" "1:\tv_max3_u32 %[t0], v224, v225, v226\n\t" "v_max3_u32 %[t1], v227, v228, v229\n\t" "v_max3_u32 %[t0], %[t0], v230, v231\n\t"  \
        "v_max3_u32 %[t1], %[t1], v232, v233\n\t" "v_max3_u32 %[t0], %[t0], v234, v235\n\t" "v_max3_u32 %[t1], %[t1], v236, v237\n\t" "v_max3_u32 %[t0], %[t0], v238, v239\n\t"  \
        "v_max3_u32 %[t1], %[t1], v240, v241\n\t" "v_max3_u32 %[t0], %[t0], v242, v243\n\t" "v_max3_u32 %[t1], %[t1], v244, v245\n\t" "v_max3_u32 %[t0], %[t0], v246, v247\n\t"  \
        "v_max3_u32 %[t1], %[t1], v248, v249\n\t" "v_max3_u32 %[t0], %[t0], v250, v251\n\t" "v_max3_u32 %[t1], %[t1], v252, v253\n\t" "v_max3_u32 %[t0], %[t0], v254, v255\n\t"  \
        "v_max_u32 %[t0], %[t0], %[t1]\n\t" "v_cmp_eq_u32 vcc, -1, %[t0]\n\t" "s_cbranch_vccz 2f\n\t" "buffer_load_dwordx4 v[224:227], %[ko], %[rw], 0 offen sc1\n\t"  \
        "buffer_load_dwordx4 v[228:231], %[ko], %[rw], 0 offen offset:1792 sc1\n\t" "buffer_load_dwordx4 v[232:235], %[ko], %[rw], 0 offen offset:3584 sc1\n\t"  \
        "buffer_load_dwordx4 v[236:239], %[ko], %[rw], %[s1] offen sc1\n\t" "buffer_load_dwordx4 v[240:243], %[ko], %[rw], %[s1] offen offset:1792 sc1\n\t"  \
        "buffer_load_dwordx4 v[244:247], %[ko], %[rw], %[s1] offen offset:3584 sc1\n\t" "buffer_load_dwordx4 v[248:251], %[ko], %[rw], %[s2] offen sc1\n\t"  \
        "buffer_load_dwordx4 v[252:255], %[ko], %[rw], %[s2] offen offset:1792 sc1\n\t" "s_waitcnt vmcnt(0)\n\t" "s_sub_u32 %[it], %[it], 1\n\t" "s_cbranch_scc0 1b\n\t" "2:" \
        : "+{v[224:239]}"(ka), "+{v[240:255]}"(kb), [t0] "=&v"(t0), [t1] "=&v"(t1), [it] "=&s"(it) \
        : [ko] "v"(ko), [rw] "s"(c.rw), [s1] "s"(3 * KVB), [s2] "s"(6 * KVB), [sp] "n"(SPINS) : "memory", "vcc", "scc")
#define O_ISSUE0() asm volatile( \
        "buffer_load_dwordx4 v[176:179], %[fo], %[rw], %[so] offen sc1\n\t" "buffer_load_dwordx4 v[180:183], %[fo], %[rw], %[so] offen offset:1024 sc1\n\t"  \
        "buffer_load_dwordx4 v[184:187], %[fo], %[rw], %[so] offen offset:2048 sc1\n\t" "buffer_load_dwordx4 v[188:191], %[fo], %[rw], %[so] offen offset:3072 sc1\n\t"  \
        "buffer_load_dwordx4 v[192:195], %[ro], %[rw], %[so] offen sc1\n\t" "" \
        : "=&{v[176:191]}"(of[0]), "=&{v[192:195]}"(og[0]) : [fo] "v"(fo), [ro] "v"(ro), [rw] "s"(c.rw), [so] "s"(so[0]) : "memory")
#define O_GET0() asm volatile( \
        "s_mov_b32 %[it], %[sp]\n\t" "s_waitcnt vmcnt(15)\n\t" "1:\tv_max3_u32 %[t0], v176, v177, v178\n\t" "v_max3_u32 %[t1], v179, v180, v181\n\t" "v_max3_u32 %[t0], %[t0], v182, v183\n\t"  \
        "v_max3_u32 %[t1], %[t1], v184, v185\n\t" "v_max3_u32 %[t0], %[t0], v186, v187\n\t" "v_max3_u32 %[t1], %[t1], v188, v189\n\t" "v_max3_u32 %[t0], %[t0], v190, v191\n\t"  \
        "v_max3_u32 %[t1], %[t1], v192, v193\n\t" "v_max3_u32 %[t0], %[t0], v194, v195\n\t" "v_max_u32 %[t0], %[t0], %[t1]\n\t" "v_cmp_eq_u32 vcc, -1, %[t0]\n\t" "s_cbranch_vccz 2f\n\t"  \
        "buffer_load_dwordx4 v[176:179], %[fo], %[rw], %[so] offen sc1\n\t" "buffer_load_dwordx4 v[180:183], %[fo], %[rw], %[so] offen offset:1024 sc1\n\t"  \
        "buffer_load_dwordx4 v[184:187], %[fo], %[rw], %[so] offen offset:2048 sc1\n\t" "buffer_load_dwordx4 v[188:191], %[fo], %[rw], %[so] offen offset:3072 sc1\n\t"  \
        "buffer_load_dwordx4 v[192:195], %[ro], %[rw], %[so] offen sc1\n\t" "s_waitcnt vmcnt(0)\n\t" "s_sub_u32 %[it], %[it], 1\n\t" "s_cbranch_scc0 1b\n\t" "2:" \
        : "+{v[176:191]}"(of[0]), "+{v[192:195]}"(og[0]), [t0] "=&v"(t0), [t1] "=&v"(t1), [it] "=&s"(it) \
        : [fo] "v"(fo), [ro] "v"(ro), [rw] "s"(c.rw), [so] "s"(so[0]), [sp] "n"(SPINS) : "memory", "vcc", "scc")
#define O_ISSUE1() asm volatile( \
        "buffer_load_dwordx4 v[196:199], %[fo], %[rw], %[so] offen sc1\n\t" "buffer_load_dwordx4 v[200:203], %[fo], %[rw], %[so] offen offset:1024 sc1\n\t"  \
        "buffer_load_dwordx4 v[204:207], %[fo], %[rw], %[so] offen offset:2048 sc1\n\t" "buffer_load_dwordx4 v[208:211], %[fo], %[rw], %[so] offen offset:3072 sc1\n\t"  \
        "buffer_load_dwordx4 v[212:215], %[ro], %[rw], %[so] offen sc1\n\t" "" \
        : "=&{v[196:211]}"(of[1]), "=&{v[212:215]}"(og[1]) : [fo] "v"(fo), [ro] "v"(ro), [rw] "s"(c.rw), [so] "s"(so[1]) : "memory")
#define O_GET1() asm volatile( \
        "s_mov_b32 %[it], %[sp]\n\t" "s_waitcnt vmcnt(10)\n\t" "1:\tv_max3_u32 %[t0], v196, v197, v198\n\t" "v_max3_u32 %[t1], v199, v200, v201\n\t" "v_max3_u32 %[t0], %[t0], v202, v203\n\t"  \
        "v_max3_u32 %[t1], %[t1], v204, v205\n\t" "v_max3_u32 %[t0], %[t0], v206, v207\n\t" "v_max3_u32 %[t1], %[t1], v208, v209\n\t" "v_max3_u32 %[t0], %[t0], v210, v211\n\t"  \
        "v_max3_u32 %[t1], %[t1], v212, v213\n\t" "v_max3_u32 %[t0], %[t0], v214, v215\n\t" "v_max_u32 %[t0], %[t0], %[t1]\n\t" "v_cmp_eq_u32 vcc, -1, %[t0]\n\t" "s_cbranch_vccz 2f\n\t"  \
        "buffer_load_dwordx4 v[196:199], %[fo], %[rw], %[so] offen sc1\n\t" "buffer_load_dwordx4 v[200:203], %[fo], %[rw], %[so] offen offset:1024 sc1\n\t"  \
        "buffer_load_dwordx4 v[204:207], %[fo], %[rw], %[so] offen offset:2048 sc1\n\t" "buffer_load_dwordx4 v[208:211], %[fo], %[rw], %[so] offen offset:3072 sc1\n\t"  \
        "buffer_load_dwordx4 v[212:215], %[ro], %[rw], %[so] offen sc1\n\t" "s_waitcnt vmcnt(0)\n\t" "s_sub_u32 %[it], %[it], 1\n\t" "s_cbranch_scc0 1b\n\t" "2:" \
        : "+{v[196:211]}"(of[1]), "+{v[212:215]}"(og[1]), [t0] "=&v"(t0), [t1] "=&v"(t1), [it] "=&s"(it) \
        : [fo] "v"(fo), [ro] "v"(ro), [rw] "s"(c.rw), [so] "s"(so[1]), [sp] "n"(SPINS) : "memory", "vcc", "scc")
#define O_ISSUE2() asm volatile( \
        "buffer_load_dwordx4 v[216:219], %[fo], %[rw], %[so] offen sc1\n\t" "buffer_load_dwordx4 v[220:223], %[fo], %[rw], %[so] offen offset:1024 sc1\n\t"  \
        "buffer_load_dwordx4 v[224:227], %[fo], %[rw], %[so] offen offset:2048 sc1\n\t" "buffer_load_dwordx4 v[228:231], %[fo], %[rw], %[so] offen offset:3072 sc1\n\t"  \
        "buffer_load_dwordx4 v[232:235], %[ro], %[rw], %[so] offen sc1\n\t" "" \
        : "=&{v[216:231]}"(of[2]), "=&{v[232:235]}"(og[2]) : [fo] "v"(fo), [ro] "v"(ro), [rw] "s"(c.rw), [so] "s"(so[2]) : "memory")
#define O_GET2() asm volatile( \
        "s_mov_b32 %[it], %[sp]\n\t" "s_waitcnt vmcnt(5)\n\t" "1:\tv_max3_u32 %[t0], v216, v217, v218\n\t" "v_max3_u32 %[t1], v219, v220, v221\n\t" "v_max3_u32 %[t0], %[t0], v222, v223\n\t"  \
        "v_max3_u32 %[t1], %[t1], v224, v225\n\t" "v_max3_u32 %[t0], %[t0], v226, v227\n\t" "v_max3_u32 %[t1], %[t1], v228, v229\n\t" "v_max3_u32 %[t0], %[t0], v230, v231\n\t"  \
        "v_max3_u32 %[t1], %[t1], v232, v233\n\t" "v_max3_u32 %[t0], %[t0], v234, v235\n\t" "v_max_u32 %[t0], %[t0], %[t1]\n\t" "v_cmp_eq_u32 vcc, -1, %[t0]\n\t" "s_cbranch_vccz 2f\n\t"  \
        "buffer_load_dwordx4 v[216:219], %[fo], %[rw], %[so] offen sc1\n\t" "buffer_load_dwordx4 v[220:223], %[fo], %[rw], %[so] offen offset:1024 sc1\n\t"  \
        "buffer_load_dwordx4 v[224:227], %[fo], %[rw], %[so] offen offset:2048 sc1\n\t" "buffer_load_dwordx4 v[228:231], %[fo], %[rw], %[so] offen offset:3072 sc1\n\t"  \
        "buffer_load_dwordx4 v[232:235], %[ro], %[rw], %[so] offen sc1\n\t" "s_waitcnt vmcnt(0)\n\t" "s_sub_u32 %[it], %[it], 1\n\t" "s_cbranch_scc0 1b\n\t" "2:" \
        : "+{v[216:231]}"(of[2]), "+{v[232:235]}"(og[2]), [t0] "=&v"(t0), [t1] "=&v"(t1), [it] "=&s"(it) \
        : [fo] "v"(fo), [ro] "v"(ro), [rw] "s"(c.rw), [so] "s"(so[2]), [sp] "n"(SPINS) : "memory", "vcc", "scc")
#define O_ISSUE3() asm volatile( \
        "buffer_load_dwordx4 v[236:239], %[fo], %[rw], %[so] offen sc1\n\t" "buffer_load_dwordx4 v[240:243], %[fo], %[rw], %[so] offen offset:1024 sc1\n\t"  \
        "buffer_load_dwordx4 v[244:247], %[fo], %[rw], %[so] offen offset:2048 sc1\n\t" "buffer_load_dwordx4 v[248:251], %[fo], %[rw], %[so] offen offset:3072 sc1\n\t"  \
        "buffer_load_dwordx4 v[252:255], %[ro], %[rw], %[so] offen sc1\n\t" "" \
        : "=&{v[236:251]}"(of[3]), "=&{v[252:255]}"(og[3]) : [fo] "v"(fo), [ro] "v"(ro), [rw] "s"(c.rw), [so] "s"(so[3]) : "memory")
#define O_GET3() asm volatile( \
        "s_mov_b32 %[it], %[sp]\n\t" "s_waitcnt vmcnt(0)\n\t" "1:\tv_max3_u32 %[t0], v236, v237, v238\n\t" "v_max3_u32 %[t1], v239, v240, v241\n\t" "v_max3_u32 %[t0], %[t0], v242, v243\n\t"  \
        "v_max3_u32 %[t1], %[t1], v244, v245\n\t" "v_max3_u32 %[t0], %[t0], v246, v247\n\t" "v_max3_u32 %[t1], %[t1], v248, v249\n\t" "v_max3_u32 %[t0], %[t0], v250, v251\n\t"  \
        "v_max3_u32 %[t1], %[t1], v252, v253\n\t" "v_max3_u32 %[t0], %[t0], v254, v255\n\t" "v_max_u32 %[t0], %[t0], %[t1]\n\t" "v_cmp_eq_u32 vcc, -1, %[t0]\n\t" "s_cbranch_vccz 2f\n\t"  \
        "buffer_load_dwordx4 v[236:239], %[fo], %[rw], %[so] offen sc1\n\t" "buffer_load_dwordx4 v[240:243], %[fo], %[rw], %[so] offen offset:1024 sc1\n\t"  \
        "buffer_load_dwordx4 v[244:247], %[fo], %[rw], %[so] offen offset:2048 sc1\n\t" "buffer_load_dwordx4 v[248:251], %[fo], %[rw], %[so] offen offset:3072 sc1\n\t"  \
        "buffer_load_dwordx4 v[252:255], %[ro], %[rw], %[so] offen sc1\n\t" "s_waitcnt vmcnt(0)\n\t" "s_sub_u32 %[it], %[it], 1\n\t" "s_cbranch_scc0 1b\n\t" "2:" \
        : "+{v[236:251]}"(of[3]), "+{v[252:255]}"(og[3]), [t0] "=&v"(t0), [t1] "=&v"(t1), [it] "=&s"(it) \
        : [fo] "v"(fo), [ro] "v"(ro), [rw] "s"(c.rw), [so] "s"(so[3]), [sp] "n"(SPINS) : "memory", "vcc", "scc")
template <int CTRL, int RM> DI float dpp_add(float v) { return v + __builtin_bit_cast(float, __builtin_amdgcn_update_dpp(0, __builtin_bit_cast(int, v), CTRL, RM, 0xf, false)); }
DI float wave_sum(float v) {
  v = dpp_add<0x111, 0xf>(v); v = dpp_add<0x112, 0xf>(v); v = dpp_add<0x114, 0xf>(v); v = dpp_add<0x118, 0xf>(v);
  v = dpp_add<0x142, 0xa>(v); v = dpp_add<0x143, 0xc>(v);
  return __builtin_bit_cast(float, __builtin_amdgcn_readlane(__builtin_bit_cast(int, v), 63));
}

struct P {
  const u16 *x, *wqkv, *wphi, *wo, *w1, *b1, *w2, *b2;
  u16* out;
  char* ws;
};

DI void range20(const f32x4 (&a)[5], float& mx, float& mn) {
  float m0 = __builtin_fmaxf(a[0][0], a[0][1]), m1 = __builtin_fmaxf(a[0][2], a[0][3]), n0 = __builtin_fminf(a[0][0], a[0][1]), n1 = __builtin_fminf(a[0][2], a[0][3]);
#pragma unroll
  for (int i = 1; i < 5; ++i) {
    m0 = __builtin_fmaxf(__builtin_fmaxf(m0, a[i][0]), a[i][1]); m1 = __builtin_fmaxf(__builtin_fmaxf(m1, a[i][2]), a[i][3]);
    n0 = __builtin_fminf(__builtin_fminf(n0, a[i][0]), a[i][1]); n1 = __builtin_fminf(__builtin_fminf(n1, a[i][2]), a[i][3]);
  }
  mx = __builtin_fmaxf(m0, m1); mn = __builtin_fminf(n0, n1);
}
DI f32x2 lin2(const f32x4 (&a)[5], int i, float k, float c) { return __builtin_elementwise_fma(f32x2{a[i >> 1][(i & 1) * 2], a[i >> 1][(i & 1) * 2 + 1]}, f32x2{k, k}, f32x2{c, c}); }
DI void swap32(unsigned& a, unsigned& b) { const v2u r = __builtin_amdgcn_permlane32_swap(a, b, false, false); a = r.x; b = r.y; }
DI void swap16(unsigned& a, unsigned& b) { const v2u r = __builtin_amdgcn_permlane16_swap(a, b, false, false); a = r.x; b = r.y; }
DI void soft20(f32x2 (&e)[10], unsigned* w) {
  SB();
#pragma unroll
  for (int i = 0; i < 10; ++i) e[i] = f32x2{__builtin_amdgcn_exp2f(e[i].x), __builtin_amdgcn_exp2f(e[i].y)};
  SB();
  const f32x2 s = (((e[0] + e[1]) + (e[2] + e[3])) + ((e[4] + e[5]) + (e[6] + e[7]))) + (e[8] + e[9]);
  const float r = __builtin_amdgcn_rcpf(s.x + s.y);
#pragma unroll
  for (int i = 0; i < 10; ++i) { const f32x2 v = e[i] * f32x2{r, r}; w[i] = pk(v.x, v.y); }
}
struct Ctx {
  LAS char* L; rsrc_t rw; unsigned tag;
  int b, panel, cg, w, lane, rt, ct, head, tb, kb;
  const char* wp[4]; u64 wadv; const char* xb; unsigned xo0, xo1; int pitch, kst;
  v4u sf[4]; unsigned sdst;
};
DI void piece(Ctx& c, int i, int dx, int dw) {
  if (i == 0) {
    const int d = c.kst == 9 ? -9 : 1;
    c.kst += d; c.xb += d * 128; c.wadv = (long long)c.pitch * (d * 64);
  }
  if (i < 2) {
    asm volatile("s_add_i32 m0, %2, %3\n\ts_nop 0\n\tglobal_load_lds_dwordx4 %0, %1" ::"v"(i ? c.xo1 : c.xo0), "s"(c.xb), "s"(dx), "n"(i * 1024) : "memory", "scc");
  } else {
    asm volatile("s_add_i32 m0, %2, %3\n\tv_lshl_add_u64 %0, %0, 0, %1\n\tglobal_load_lds_dwordx4 %0, off" : "+v"(c.wp[i - 2]) : "v"(c.wadv), "s"(dw), "n"((i - 2) * WR) : "memory", "scc");
  }
}
DI int img_col(int cg, int ch) {
  const int win = ((160 * (cg & 7)) & ~63) >> 1, j = cg - 8;
  const int b0 = cg < 8 ? D + win : win;
  const int b1 = cg < 8 ? 2 * D + win - 96 : 3 * D + (j < 7 ? 512 + 160 * j : 1600) - 96;
  const int b2 = cg < 8 ? 3 * D + 64 * cg - 192 : b1;
  const int cc = j >= 6 && ch > 27 ? 27 : ch;
  return (ch < 12 ? b0 : ch < 24 ? b1 : b2) + 8 * cc;
}

template <int NP, int NN, bool ISB, class TF>
DI void gemm1(const P& p, Ctx& c, int pbase, int nbase, f32x4 (&acc)[2][NP + NN], v4u (&wpre)[7][5], float (&mb)[2], float (&rs)[2], TF&& tail) {
  constexpr int NT = NP + NN;
  LAS char* L = c.L;
  const int lane = c.lane, w = c.w, g = lane >> 4, tok = lane & 15;
  const int offN = XB + (lane >> 2) * WR + 8 * (lane & 3) + nbase, offP = XB + (lane >> 2) * WR + 40 * (lane & 3) + pbase;
  const int xr = (w & 3) * 32 + tok;
  const int offX = xr * 128 + 16 * (g ^ ((xr >> 1) & 7));
  v2u wf[NT][2]; v4u xf[2][2];
  LAS char *aN, *aP, *aX0, *aX1;
  auto base = [&](int bo) { aN = L + offN + bo; aP = L + offP + bo; aX0 = L + offX + bo; aX1 = L + (offX ^ 64) + bo; asm volatile("" : "+v"(aN), "+v"(aP), "+v"(aX0), "+v"(aX1)); };
  auto rd = [&](int s, int t, int h) { wf[t][h] = trd((t < NP ? aP + t * 8 : aN + (t - NP) * 32) + s * 512 + h * 16 * WR); };
  auto rdx = [&](int s, int a) { xf[s][a] = *(LAS v4u*)((s ? aX1 : aX0) + a * 2048); };
  auto mm = [&](int s, int t, int a, bool first = false) {
    const v4u f = {wf[t][0].x, wf[t][0].y, wf[t][1].x, wf[t][1].y};
    if (first) asm volatile("v_mfma_f32_16x16x32_bf16 %0, %1, %2, 0" : "=&v"(acc[a][t]) : "v"(f), "v"(xf[s][a]));
    else asm volatile("v_mfma_f32_16x16x32_bf16 %0, %1, %2, %0" : "+v"(acc[a][t]) : "v"(f), "v"(xf[s][a]));
  };
  constexpr int PR1 = 4, PR2 = 8, PR3 = NT + 4;
  auto prio = [&](int n) { if (n == 0) __builtin_amdgcn_s_setprio(3); else if (n == PR1) __builtin_amdgcn_s_setprio(2); else if (n == PR2) __builtin_amdgcn_s_setprio(1); else if (n == PR3) __builtin_amdgcn_s_setprio(0); };
  auto k1b = [&](auto&& hook) {
#pragma unroll
    for (int t = 2; t < NT; ++t) {
      prio(t - 2);
      mm(1, t, 0);
      if (t == 2) { rdx(0, 0); rdx(0, 1); } else if (t == 3) { rd(0, 1, 0); rd(0, 1, 1); } else rd(0, t - 1, 0);
      mm(1, t, 1);
      if (t == 2) { rd(0, 0, 0); rd(0, 0, 1); } else if (t == 3) { rd(0, 2, 0); rd(0, 2, 1); } else rd(0, t - 1, 1);
      hook(t - 2);
    }
  };
  auto k0 = [&](auto&& hook, bool first = false) {
#pragma unroll
    for (int t = 0; t < NT; ++t) {
      prio(NT - 2 + t);
      mm(0, t, 0, first);
      if (t) rd(1, t - 1, 0); else if (!first) rd(0, NT - 1, 0);
      if (t == 1 || t == 2) rdx(1, t - 1);
      mm(0, t, 1, first);
      if (t) rd(1, t - 1, 1); else if (!first) rd(0, NT - 1, 1);
      hook(NT - 2 + t);
    }
    mm(1, 0, 0); rd(1, NT - 1, 0); mm(1, 0, 1); rd(1, NT - 1, 1);
  };
  auto close = [&]() { mm(1, 1, 0); mm(1, 1, 1); WAITL0(); BAR(); };
  int bo = 0, dx = 2 * BUF + w * 2048, dw = 2 * BUF + XB + w * 4 * WR;
#define RH 0, 2, 4, 6, 8, 10
  constexpr int rh[6] = {RH};
  auto ring = [&](int n) {
#pragma unroll
    for (int i = 0; i < 6; ++i) if (n == rh[i]) piece(c, i, dx, dw);
  };
  base(0);
  rdx(0, 0); rdx(0, 1);
#pragma unroll
  for (int t = 0; t < NT; ++t) { rd(0, t, 0); rd(0, t, 1); }
  k0([&](int n) {
    constexpr int R = 12 - NP1, E = R > 7 ? R - 7 : 0;
    const int h = n - (NT - 2), m0 = NP1 + (h < E ? 2 * h : E + h);
#pragma unroll
    for (int m = m0; m < m0 + (h < E ? 2 : 1); ++m) {
      if (m < 6) piece(c, m, BUF + w * 2048, BUF + XB + w * 4 * WR);
      else if (m < 12) piece(c, m - 6, dx, dw);
    }
  }, true);
  auto pub = [&]() {
  if (!ISB && w < 3) {
#pragma unroll
    for (int t = 0; t < 4; ++t) st16(c.rw, c.sdst + t * NKS * 1024, c.sf[t]);
  } else if (!ISB) {
    LAS float* q = (LAS float*)(L + L_CP) + lane;
    st4(c.rw, c.b < 228 ? WS_CS + ((c.b % 57) * 64 + lane) * 16 + (c.b / 57) * 4 : OOB, __builtin_bit_cast(unsigned, ((q[0] + q[64]) + (q[128] + q[192])) + q[256]));
    st4(c.rw, lane < 8 ? WS_RS + (c.panel * 128 + c.cg * 8 + lane) * 4 : OOB, *(LAS unsigned*)(L + L_ST + (lane & 7) * 4));
  }
  };
  auto sen = [&](int n) {
    const unsigned d = n < 10 ? WS_AFR + ((c.b >> 1) * NKS + 54 + 10 * (c.b & 1) + n) * 1024 : WS_KVP + c.b * (2 * KVB) + (n - 10) * 1024;
    __builtin_amdgcn_raw_buffer_store_b128(v4u{~0u, ~0u, ~0u, ~0u}, c.rw, n < 13 || (n == 13 && lane < 32) ? lane * 16 : OOB, d, 16);
  };
  pub();
  if (ISB) { sen(3 * w - 12); sen(3 * w - 11); sen(3 * w - 10); WAITV(9); }
  else { if (w == 3) { sen(12); sen(13); } WAITV(10); }
  const unsigned fo = FL_PT + lane * 16;
  const unsigned po = lane == 0 ? FL_PT + c.b * 4 : OOB;
  v4u fl = {c.tag, c.tag, c.tag, c.tag};
#pragma unroll 1
  for (int s = 0; s < 7; ++s) {
    dx += bo - (bo == 0 ? 2 * BUF : bo - BUF); dw += bo - (bo == 0 ? 2 * BUF : bo - BUF);
    bo = bo == 2 * BUF ? 0 : bo + BUF;
    base(bo);
    close();
    k1b([&](int n) {
      if (n == 0 && ISB) asm volatile("s_cmp_lg_u32 %4, %6\n\ts_cbranch_scc1 1f\n\tbuffer_store_dword %5, %2, %3, 0 offen sc1\n1:\ts_cmp_lg_u32 %4, 6\n\ts_cbranch_scc1 2f\n\tbuffer_load_dwordx4 %0, %1, %3, 0 offen sc1\n2:"
                               : "+v"(fl) : "v"(fo), "v"(po), "s"(c.rw), "s"(s + 8 * (w - 4)), "v"(c.tag), "n"(FLS) : "memory", "scc");
      ring(n);
    });
    k0(ring);
    WAITV(6);
  }
  const unsigned wo = WS_WT + (c.ct * 5 * NKS + (ISB ? 38 + 4 * w : c.kb)) * 1024 + lane * 16;
  constexpr int NW8 = ISB ? 10 : 2, NW9 = ISB ? 10 : 3;
  auto wl = [&](int n) { wpre[n / 5][n % 5] = ld16(c.rw, wo + ((n % 5) * NKS + n / 5) * 1024); };
  if (ISB) {
    asm volatile("" : "+v"(fl));
#pragma unroll 1
    for (unsigned i = 0; i < SPINS && __builtin_amdgcn_ballot_w64(fl.x != c.tag || fl.y != c.tag || fl.z != c.tag || fl.w != c.tag); ++i) { asm volatile("" ::: "memory"); fl = ld16(c.rw, fo); }
    if (c.b == 0 && w == 4) st4(c.rw, lane == 0 ? WS_EPOCH : OOB, c.tag);
  }
  base(2 * BUF);
  close();
  {
    const int tid = w * 64 + lane;
    const int col = img_col(c.cg, (tid >> 3) & 31) + (tid & 7);
    f32x4 cq; unsigned bb;
    k1b([&](int n) {
      if (n == 0) {
        if (ISB) asm volatile("buffer_load_dword %0, %1, %2, 0 offen sc1" : "=v"(bb) : "v"(WS_RS + (c.panel * 128 + (w & 3) * 32 + (lane & 31)) * 4), "s"(c.rw) : "memory");
        else asm volatile("buffer_load_dwordx4 %0, %2, %3, 0 offen sc1\n\tglobal_load_ushort %1, %4, %5" : "=&v"(cq), "=&v"(bb) : "v"(WS_CS + col * 16), "s"(c.rw), "v"((col >= 3 * D ? col - 3 * D : 0) * 2), "s"(p.b1) : "memory");
      } else if (n <= NW8) wl(n - 1);
    });
    k0([&](int n) { if (n <= NW8) wl(n - 1); });
    if (ISB) {
      asm volatile("s_waitcnt vmcnt(%1)" : "+v"(bb) : "n"(NW8) : "memory");
      *(LAS unsigned*)(L + L_RS + ((w & 3) * 32 + (lane & 31)) * 4) = bb;
    } else {
      asm volatile("s_waitcnt vmcnt(%2)" : "+v"(cq), "+v"(bb) : "n"(NW8) : "memory");
      *(LAS float*)(L + L_CS + tid * 4) = (cq[0] + cq[1]) + (cq[2] + cq[3]);
      *(LAS float*)(L + L_B1 + tid * 4) = lo(bb);
    }
    base(0);
    close();
  }
  {
    unsigned r0, r1;
    auto h9 = [&](int n) {
      if (n == 1) { r0 = *(LAS unsigned*)(L + L_RS + xr * 4); r1 = *(LAS unsigned*)(L + L_RS + xr * 4 + 64); }
      if (n == 6) {
        mb[0] = -lo(r0); rs[0] = __builtin_amdgcn_rcpf(hi(r0)); mb[1] = -lo(r1); rs[1] = __builtin_amdgcn_rcpf(hi(r1));
        asm volatile("" : "+v"(mb[0]), "+v"(mb[1]), "+v"(rs[0]), "+v"(rs[1]));
      }
      if (n < NW9) wl(NW8 + n);
      else if (!ISB && n < NW9 + 3) {
        const int ch = (n - NW9) * 64 + lane, r = (ch * 205) >> 11;
        const char* q = ch < 160 ? (const char*)p.x + (size_t)((c.rt ^ 1) * 64 + w * 16) * (D * 2) + (r * (D * 2 - 160) + ch * 16) : (const char*)p.b2 + (ch < 170 ? ch - 160 : 0) * 16;
        glds(q + c.ct * 160, L_RES + w * 3072 + (n - NW9) * 1024);
      }
    };
    k1b(h9);
    k0(h9);
#pragma unroll
    for (int t = 1; t < NT; ++t) { mm(1, t, 0); mm(1, t, 1); tail(t - 1); }
    __builtin_amdgcn_s_setprio(ISB ? PB0 : 3);
    asm volatile("s_nop 15" ::: "memory");
    SB();
  }
}
DI void hconst(Ctx& c, int pos, f32x4 (&k)[4]) {
  LAS char* t = c.L + pos * 2 + (c.lane >> 4) * 16;
  k[0] = *(LAS f32x4*)(t + L_CS); k[1] = *(LAS f32x4*)(t + L_CS + 64); k[2] = *(LAS f32x4*)(t + L_B1); k[3] = *(LAS f32x4*)(t + L_B1 + 64);
}
DI void hidden(Ctx& c, const f32x4& a00, const f32x4& a01, const f32x4& a10, const f32x4& a11, const f32x4 (&k)[4], unsigned dst, const float (&mb)[2], const float (&rs)[2]) {
#pragma unroll
  for (int a = 0; a < 2; ++a) {
    const float n = mb[a] * rs[a];
    f32x4 u = (a ? a10 : a00) * rs[a] + (k[0] * n + k[2]), v = (a ? a11 : a01) * rs[a] + (k[1] * n + k[3]);
#pragma unroll
    for (int i = 0; i < 4; ++i) { u[i] *= __builtin_fmaxf(u[i], 0.f); v[i] *= __builtin_fmaxf(v[i], 0.f); }
    st16(c.rw, dst + a * NKS * 1024, pk8(u, v));
  }
}

template <bool ISB>
DI void run(const P& p, const int w, unsigned (&cw)[4]) {
  extern __shared__ __attribute__((aligned(16))) char smem[];
  LAS char* L = (LAS char*)smem;
  Ctx c;
  const int b = blockIdx.x, xcd = b & 7, r = b >> 3;
  if (K1MAP == 0) { c.panel = xcd * 2 + (r >> 4); c.cg = r & 15; }
  else if (K1MAP == 1) { c.panel = (xcd >> 1) * 4 + (r >> 3); c.cg = (r & 3) + (xcd & 1) * 4 + ((r >> 2) & 1) * 8; }
  else { c.panel = (xcd >> 2) * 8 + (r >> 2); c.cg = (r & 1) + (xcd & 3) * 2 + ((r >> 1) & 1) * 8; }
  if (K2MAP == 0) { c.rt = xcd * 4 + (r >> 3); c.ct = r & 7; }
  else { c.rt = (xcd >> 1) * 8 + (r >> 2); c.ct = (xcd & 1) * 4 + (r & 3); }
  const int tid = threadIdx.x, lane = tid & 63;
  const int g = lane >> 4, tok = lane & 15, panel = c.panel, cg = c.cg, pid = panel * 16 + cg;
  const bool KV = cg < 8;
  c.L = L; c.b = b; c.w = w; c.lane = lane;
  c.head = 4 * xcd + (r & 3); c.tb = r >> 2;
  c.kb = ((c.rt ^ (HPAR ? c.ct : 0)) & 1 ? 26 + (c.rt & 1) * 2 : 0) + 7 * w - (w == 3 ? c.rt & 1 : 0);
  c.rw = __builtin_amdgcn_make_buffer_rsrc(p.ws, 0, WS_END, 0x00020000);
  unsigned ep; v4u xa, xc;
  {
    const char* row = (const char*)p.x + (size_t)(panel * 128 + cg * 8 + w) * (D * 2);
    asm volatile("s_load_dword %0, %1, 0" : "=s"(ep) : "s"(p.ws) : "memory");
    asm volatile("global_load_dwordx4 %0, %1, %2" : "=v"(xa) : "v"(lane * 16), "s"(row) : "memory");
    asm volatile("global_load_dwordx4 %0, %1, %2 offset:1024" : "=v"(xc) : "v"(tok * 16), "s"(row) : "memory");
  }
  const int pks0 = 3 * (b / 10) + w, pks = pks0 < NKS ? pks0 : NKS - 1, pcb = b % 10;
  {
    const char* src; unsigned o0, st;
    if (!ISB && w < 3) {
      if (pks < 54) { src = (const char*)p.w2 + (size_t)(32 * pks) * (D * 2); o0 = (lane >> 3) * (D * 2); st = 8 * D * 2; }
      else if (pks < 70) { src = (const char*)p.wo + (size_t)(40 * (pks - 54)) * (D * 2); o0 = ((lane >> 5) * 8 + ((lane >> 3) & 3)) * (D * 2); st = 20 * D * 2; }
      else { src = (const char*)p.wo + (size_t)(160 * (pks - 70) + 16) * (D * 2); o0 = ((lane >> 5) * 40 + ((lane >> 3) & 3)) * (D * 2); st = 80 * D * 2; }
      src += pcb * 128;
    } else {
      const int jb = b < 228 ? b : 0, lc = jb % 57, k0 = (jb / 57) * 160 + (w - 3) * 32;
      if (lc < 30) { src = (const char*)p.wqkv + (size_t)k0 * (3 * D * 2) + lc * 128; st = 8 * 3 * D * 2; }
      else { src = (const char*)p.w1 + (size_t)k0 * (F * 2) + (lc - 30) * 128; st = 8 * F * 2; }
      o0 = (lane >> 3) * (st >> 3);
    }
    o0 += (lane & 7) * 16;
    const unsigned st2 = (!ISB && w < 3 && pks >= 54) ? (pks < 70 ? 4 * D * 2 : 20 * D * 2) : 2 * st;
    const unsigned dst = 2 * BUF + w * 4096;
    gldso(src, o0, dst); gldso(src, o0 + st, dst + 1024); gldso(src, o0 + st2, dst + 2048); gldso(src, o0 + st2 + st, dst + 3072);
  }
  {
    const int col = img_col(cg, lane & 31), k = (lane >> 5) * 32 + (w & 3) * 8 + (w >> 2) * 4;
    const bool f1 = col >= 3 * D;
    const unsigned pitch = f1 ? F * 2 : 3 * D * 2;
    const char* q = (f1 ? (const char*)p.w1 - 3 * D * 2 : (const char*)p.wqkv) + (k * pitch + col * 2);
    constexpr int ro[4] = {ROT};
    const int ri = (panel + cg) & 3;
    c.kst = (ri == 0 ? ro[0] : ri == 1 ? ro[1] : ri == 2 ? ro[2] : ro[3]) - 1; c.pitch = pitch;
#pragma unroll
    for (int i = 0; i < 4; ++i) c.wp[i] = q + (i + c.kst * 64) * (int)pitch;
    c.xb = (const char*)p.x + (size_t)(panel * 128 + w * 16) * (D * 2) + c.kst * 128;
    c.xo0 = (lane >> 3) * (D * 2) + 16 * ((lane & 7) ^ (lane >> 4)); c.xo1 = (8 + (lane >> 3)) * (D * 2) + 16 * ((lane & 7) ^ (4 | (lane >> 4)));
  }
#pragma unroll
  for (int i = 0; i < 6; ++i) piece(c, i, w * 2048, XB + w * 4 * WR);
  if (ISB && w == 7) *(LAS v4u*)(L + L_ZERO + lane * 16) = v4u{0, 0, 0, 0};
  if (!ISB && tid < 16) *(LAS unsigned*)(L + L_CNT + tid * 4) = 0;
  asm volatile("s_waitcnt vmcnt(6) lgkmcnt(0)" : "+s"(ep), "+v"(xa), "+v"(xc), "+s"(cw[0]), "+s"(cw[1]), "+s"(cw[2]), "+s"(cw[3])::"memory");
#pragma unroll
  for (int t = 0; t < 4; ++t) c.sf[t] = trd2(L + 2 * BUF + w * 4096 + (lane >> 2) * 128 + (lane & 3) * 8 + t * 32, 16 * 128);
  c.tag = ep + 1u;
  c.sdst = pks0 < NKS && b < NWT ? WS_WT + (4 * pcb * NKS + pks) * 1024 + lane * 16 : OOB;
#pragma unroll
  for (int i = 0; i < NP0; ++i) piece(c, i, BUF + w * 2048, BUF + XB + w * 4 * WR);
  {
    if (lane >= 16) xc = v4u{0, 0, 0, 0};
    float s0 = 0.f, s1 = 0.f, s2 = 0.f, s3 = 0.f;
#pragma unroll
    for (int i = 0; i < 4; ++i) { s0 = dot2(xa[i], 0x3f803f80u, s0); s1 = dot2(xa[i], xa[i], s1); s2 = dot2(xc[i], 0x3f803f80u, s2); s3 = dot2(xc[i], xc[i], s3); }
    s0 = wave_sum(s0 + s2) * (1.f / D); s1 = wave_sum(s1 + s3) * (1.f / D);
    *(LAS unsigned*)(L + L_ST + w * 4) = pk(s0, __builtin_sqrtf(rb(rb(__builtin_fmaxf(s1 - s0 * s0, 0.f)) + 1e-5f)));
  }
  if (ISB || w == 3) {
    const v4u ones = {0x3f803f80u, 0x3f803f80u, 0x3f803f80u, 0x3f803f80u};
    f32x4 cs[4];
#pragma unroll
    for (int t = 0; t < 4; ++t) cs[t] = mfma32(c.sf[t], ones, f32x4{0, 0, 0, 0});
    const f32x4 v = tok == 0 ? cs[0] : tok == 1 ? cs[1] : tok == 2 ? cs[2] : cs[3];
    if (tok < 4) *(LAS f32x4*)(L + L_CP + (w - 3) * 256 + tok * 64 + g * 16) = v;
  }
  SB();
#pragma unroll
  for (int i = NP0; i < NP1; ++i) piece(c, i, BUF + w * 2048, BUF + XB + w * 4 * WR);
  if (ISB && lane < 50) gldso((const char*)p.wphi + (cg & 7) * 3200 + (w - 4) * 800, lane * 16, L_WPHI + (w - 4) * 800);
  asm volatile("s_waitcnt vmcnt(%0)" ::"n"(NP1 + ISB) : "memory");
  WAITL0(); BAR(); SB();

  v4u wpre[7][5];
  float mb[2], rs[2];
  const int dd = (cg & 1) * 32, rt = c.rt, ct = c.ct, aw = w & 3;
  const unsigned afr = WS_AFR + rt * 4 * NKS * 1024 + lane * 16, wt = WS_WT + ct * 5 * NKS * 1024 + lane * 16;
  const unsigned fh = sel(lane < 16, MB_H + (panel * 16 + lane) * 256 + cg * 4, OOB);
  const unsigned hd0 = WS_AFR + (panel * 8 + aw * 2) * NKS * 1024 + lane * 16;
  auto pack = [&](f32x4 (&v)[7], v4u (&bq)[3]) {
    bq[0] = pk8(v[0], v[1]); bq[1] = pk8(v[2], v[3]);
    const v2u e = pk4(v[4]); bq[2] = v4u{e.x, e.y, 0x00003f80u, 0u};
  };
  if (!ISB) {
    if (!KV) {
      f32x4 acc[2][8], hk[4][4];
      unsigned hd, hl;
      gemm1<0, 8, false>(p, c, 0, 256, acc, wpre, mb, rs, [&](int n) {
        if (n < 4) hconst(c, 256 + 64 * n, hk[n]);
        if (n == 4) { hd = hd0 + (cg < 15 ? 5 * cg - 23 : 51) * 1024; hl = cg < 14 ? hd : OOB; asm volatile("" : "+v"(hd), "+v"(hl)); }
      });
#pragma unroll
      for (int u = 0; u < 4; ++u) hidden(c, acc[0][2 * u], acc[0][2 * u + 1], acc[1][2 * u], acc[1][2 * u + 1], hk[u], (u < 3 ? hd : hl) + u * 1024, mb, rs);
    } else {
      f32x4 acc[2][7], hk[4], cs[5];
      unsigned hd; LAS char* d;
      gemm1<5, 2, false>(p, c, 192 + dd, 384, acc, wpre, mb, rs, [&](int n) {
        if (n == 0) hconst(c, 384, hk);
        if (n == 1) {
#pragma unroll
          for (int t = 0; t < 5; ++t) cs[t] = *(LAS f32x4*)(L + L_CS + (192 + dd) * 2 + g * 80 + t * 16);
        }
        if (n == 2) { hd = hd0 + 2 * cg * 1024; d = L + L_V + (w * 32 + tok) * PV + g * 48; asm volatile("" : "+v"(hd), "+v"(d)); }
      });
      hidden(c, acc[0][5], acc[0][6], acc[1][5], acc[1][6], hk, hd, mb, rs);
      SB();
#pragma unroll
      for (int a = 0; a < 2; ++a) {
        v4u bq[3];
        const float n = mb[a] * rs[a];
#pragma unroll
        for (int t = 0; t < 5; ++t) acc[a][t] = acc[a][t] * rs[a] + cs[t] * n;
        pack(acc[a], bq);
        *(LAS v4u*)(d + a * 16 * PV) = bq[0]; *(LAS v4u*)(d + a * 16 * PV + 16) = bq[1]; *(LAS v4u*)(d + a * 16 * PV + 32) = bq[2];
      }
      arrive(C_V);
    }
    const bool full = w < 2 || !(rt & 1);
    const unsigned bp = wt + c.kb * 1024, bpl = full ? bp : OOB;
    unsigned ap = WS_AFR + ((rt ^ 1) * 4 * NKS + c.kb) * 1024 + lane * 16, apl = full ? ap : OOB;
    auto pld = [&](unsigned off, unsigned so) { SB(); asm volatile("s_waitcnt vmcnt(%0)" ::"n"(INF - 1)); const v4u v = ld16s(c.rw, off, so); SB(); return v; };
    SB();
    __builtin_amdgcn_s_setprio(PA0);
#pragma unroll
    for (int n = 5; n < 8; ++n) wpre[n / 5][n % 5] = ld16s(c.rw, bp, ((n % 5) * NKS + n / 5) * 1024);
    SB();
    WAITV(3);
    arrive_flag(C_H, 7, c.rw, fh, c.tag);
    const unsigned mb0 = (rt * 8 + ct) * 256;
    f32x4 acc[2][5], t[5], u[5];
    {
      v16u fv;
#pragma unroll
      for (int n = 8; n < 35; ++n) {
        if (n == 34 - FLEAD) await(L, C_H, 8);
        if (n == 35 - FLEAD) asm volatile("s_load_dwordx16 %0, %1, %2 glc" : "=s"(fv) : "s"(p.ws), "s"(MB_H + mb0) : "memory");
        wpre[n / 5][n % 5] = pld(n < 30 ? bp : bpl, ((n % 5) * NKS + n / 5) * 1024);
      }
      asm volatile("s_waitcnt lgkmcnt(0)" : "+s"(fv)::"memory");
      if (differs<16>(fv, c.tag)) wait_flags<16>(p.ws, c.rw, MB_H + mb0, 0, c.tag);
    }
    LAS char* stg = L + w * AW + lane * 16;
    LAS char* q0 = L + ((w + 3) & 3) * AW + lane * 16;
    LAS char* q1 = L + ((w + 2) & 3) * AW + 5120 + lane * 16;
    LAS char* q2 = L + ((w + 1) & 3) * AW + 10240 + lane * 16;
    const unsigned xo = WS_XCH + w * 5120 + lane * 16;
    v4u ra[RA]; v2u xv[5], bv[5];
    unsigned so[4], cn, fx;
#pragma unroll
    for (int i = 0; i < 4; ++i) so[i] = ((w + 1 + i) & 3) * NKS * 1024;
    const int dr = (rt & 1 ? 4 : -4) * NKS * 1024;
    auto ald = [&](int m) { if (m < INF) return pld(m % 7 == 6 ? apl : ap, so[m / 7 % 4] + m % 7 * 1024); SB(); const v4u v = ld16s(c.rw, m % 7 == 6 ? apl : ap, so[m / 7 % 4] + m % 7 * 1024); SB(); return v; };
#pragma unroll
    for (int m = 0; m < RA; ++m) ra[m] = ald(m);
#pragma unroll
    for (int n = 0; n < 8; ++n) {
      const int a = n & 1, i = n & 3, th = n < 4 ? 4 : 8;
#pragma unroll
      for (int k = 0; k < 7; ++k) {
#pragma unroll
        for (int j = 0; j < 5; ++j) {
          acc[a][j] = mfma32(wpre[k][j], ra[(n * 7 + k) % RA], k || i == 3 ? acc[a][j] : f32x4{0, 0, 0, 0});
          const int m = k * 5 + j, f = n * 7 + k - 1 + RA;
          if (j == LDJ && n + k && f < 56) {
            if (f == 28) { ap += dr; apl += dr; }
            ra[f % RA] = ald(f);
          }
          if (i == 3 && m == 0) { recheck(cn, C_A + 4, th); SB(); }
          if (i == 3 && m < 5) { t[j] = *(LAS f32x4*)(q1 + j * 1024); SB(); }
          if (i && m >= 5 && m < 15 && m % 2) { *(LAS f32x4*)(stg + ((i - 1) * 5 + (m - 5) / 2) * 1024) = acc[a ^ 1][(m - 5) / 2]; if (m == 13) arrive(C_A + 4 * (i - 1)); SB(); }
          if (i == 2) {
            if (m == 16) { cn = peekv(L, C_A); SB(); }
            if (m == 20) { recheck(cn, C_A, th); SB(); }
            if (k == 4) { acc[a ^ 1][j] = *(LAS f32x4*)(q0 + j * 1024); SB(); }
            if (m == 29) { cn = peekv(L, C_A + 4); SB(); }
            if (k == 3 && n == 2) { LAS char* q = L + L_RES + w * 3072 + j * 32 + g * 8; xv[j] = *(LAS v2u*)(q + tok * 160); bv[j] = *(LAS v2u*)(q + 2560); SB(); }
            if (k == 6 && n == 2) { acc[a ^ 1][j] += f32x4{lo(xv[j].x) + lo(bv[j].x), hi(xv[j].x) + hi(bv[j].x), lo(xv[j].y) + lo(bv[j].y), hi(xv[j].y) + hi(bv[j].y)}; SB(); }
          }
          if (n == 3 && m == 30) { cn = peekv(L, C_A + 8); SB(); }
          if (n == 4) {
            if (m == 0) { recheck(cn, C_A + 8, 4); SB(); }
            if (k == 0) { u[j] = *(LAS f32x4*)(q2 + j * 1024); t[j] += acc[1][j]; SB(); }
            if (k == 2) { st16(c.rw, xo + (mb0 ^ 2048) * 80 + j * 1024, __builtin_bit_cast(v4u, t[j] + u[j])); SB(); }
          }
          if (n * 7 + k == 30 + XD && j == 4) { asm volatile("s_waitcnt vmcnt(%0)" ::"n"(XD) : "memory"); st4(c.rw, sel(lane == 0, MB_X + (mb0 ^ 2048) + w * 4, OOB), c.tag); SB(); }
          if (n == 7) {
            if (m == 20) { cn = peekv(L, C_A + 8); SB(); }
            if (m == 24) { recheck(cn, C_A + 8, 8); SB(); }
            if (k == 5) { u[j] = *(LAS f32x4*)(q2 + j * 1024); SB(); }
            if (m == 29) asm volatile("s_load_dword %0, %1, %2 glc" : "=s"(fx) : "s"(p.ws), "s"(MB_X + mb0 + w * 4) : "memory");
          }
        }
      }
      SB();
    }
    asm volatile("s_waitcnt lgkmcnt(0)" : "+s"(fx)::"memory");
    if (fx != c.tag) wait_flags<1>(p.ws, c.rw, MB_X + mb0 + w * 4, 0, c.tag);
#pragma unroll
    for (int j = 0; j < 5; ++j) acc[0][j] = __builtin_bit_cast(f32x4, ld16(c.rw, xo + mb0 * 80 + j * 1024));
    SB();
#pragma unroll
    for (int j = 0; j < 5; ++j) acc[1][j] += t[j] + u[j];
    SB();
#pragma unroll
    for (int j = 0; j < 5; ++j) *(LAS f32x4*)(q0 + j * 1024) = acc[1][j] + acc[0][j];
    asm volatile("hk_end0:");
    arrive(C_X + 4 * w);
  } else {
    const int head = c.head, tb = c.tb;
    const unsigned qf = MB_Q + (head * 8 + tb) * 128 + (aw >> 1) * 4;
    {
      f32x4 acc[2][7], pq[2][5], hk[4], cs[5];
      unsigned hd; LAS char* wp;
      gemm1<5, 2, true>(p, c, dd, KV ? 448 : 192, acc, wpre, mb, rs, [&](int n) {
        if (n == 0) hconst(c, KV ? 448 : 192, hk);
        if (n == 2) {
          hd = hd0 + (KV ? 2 * cg + 1 : (cg < 15 ? 5 * cg - 24 : 50)) * 1024;
          wp = L + sel((lane & 3) == g, L_WPHI + g * 800 + ((lane & 15) >> 2) * 40, L_ZERO);
          asm volatile("" : "+v"(hd), "+v"(wp));
        }
      });
      {
        v4u a0[5], a1[5], bq[2][3]; v2u a2[5];
#pragma unroll
        for (int t = 0; t < 5; ++t) cs[t] = *(LAS f32x4*)(L + L_CS + dd * 2 + g * 80 + t * 16);
        hidden(c, acc[0][5], acc[0][6], acc[1][5], acc[1][6], hk, hd, mb, rs);
        SB();
#pragma unroll
        for (int t = 0; t < 5; ++t) { a0[t] = trd2(wp + t * 8, 160); a1[t] = trd2(wp + 320 + t * 8, 160); a2[t] = trd(wp + 640 + t * 8); }
#pragma unroll
        for (int a = 0; a < 2; ++a) {
#pragma unroll
          for (int t = 0; t < 5; ++t) acc[a][t] += cs[t] * mb[a];
          pack(acc[a], bq[a]);
        }
        SB();
#pragma unroll
        for (int a = 0; a < 2; ++a) {
#pragma unroll
          for (int t = 0; t < 5; ++t) pq[a][t] = mfma32(a0[t], bq[a][0], f32x4{0, 0, 0, 0});
#pragma unroll
          for (int t = 0; t < 5; ++t) pq[a][t] = mfma32(a1[t], bq[a][1], pq[a][t]);
#pragma unroll
          for (int t = 0; t < 5; ++t) pq[a][t] = mfma32(v4u{a2[t].x, a2[t].y, 0u, 0u}, bq[a][2], pq[a][t]);
        }
      }
      SB();
      if (KV || QH == 0) {
        WAITV(0);
        arrive_flag(C_H, 7, c.rw, fh, c.tag);
      }
      constexpr float L2E = 1.4426950408889634f;
      if (KV) {
#pragma unroll
        for (int a = 0; a < 2; ++a) {
          unsigned wv[20]; float mx, mn; f32x2 e[10];
          range20(pq[a], mx, mn);
          const float k = L2E * rs[a];
#pragma unroll
          for (int i = 0; i < 10; ++i) e[i] = lin2(pq[a], i, k, mx * -k);
          soft20(e, wv);
#pragma unroll
          for (int i = 0; i < 10; ++i) e[i] = lin2(pq[a], i, -k, mn * k);
          soft20(e, wv + 10);
          LAS char* d = L + L_PHK + ((aw * 2 + a) * 16 + tok) * PK + g * 80;
          *(LAS v4u*)(d) = v4u{wv[0], wv[1], wv[4], wv[5]};
          *(LAS v4u*)(d + 16) = v4u{wv[8], wv[9], wv[12], wv[13]};
          *(LAS v4u*)(d + 32) = v4u{wv[2], wv[3], wv[6], wv[7]};
          *(LAS v4u*)(d + 48) = v4u{wv[10], wv[11], wv[14], wv[15]};
          *(LAS v4u*)(d + 64) = v4u{wv[16], wv[17], wv[18], wv[19]};
          SB();
        }
      } else {
#pragma unroll
        for (int a = 0; a < 2; ++a) {
          unsigned wv[12]; float mx, mn;
          range20(pq[a], mx, mn);
          const float iv = __builtin_amdgcn_rcpf(__builtin_fmaxf(mx - mn, 1e-30f));
#pragma unroll
          for (int i = 0; i < 10; ++i) { const f32x2 v = lin2(pq[a], i, iv, mn * -iv); wv[i] = __builtin_bit_cast(unsigned, __builtin_amdgcn_cvt_pknorm_u16(v.x, v.y)); }
          wv[10] = wv[11] = __builtin_bit_cast(unsigned, (mx - mn) * rs[a] * (L2E / 65535.f));
          const unsigned d = WS_PHQ + (((cg - 8) * 4 + g) * 32 + panel * 2 + (aw >> 1)) * 3072 + (((aw & 1) * 2 + a) * 16 + tok) * 16;
#pragma unroll
          for (int i = 0; i < 3; ++i) st16(c.rw, d + i * 1024, v4u{wv[4 * i], wv[4 * i + 1], wv[4 * i + 2], wv[4 * i + 3]});
          SB();
          if (a + 1 == QH) { asm volatile("s_waitcnt vmcnt(%0)" ::"n"(3 * QH) : "memory"); arrive_flag(C_H, 7, c.rw, fh, c.tag); }
        }
      }
    }
    const unsigned qo = WS_PHQ + (head * 32 + tb * 4 + aw) * 3072 + lane * 16;
    if (KV) {
      arrive(C_V);
      await(L, C_V, 8);
      f32x4 kv[2][3];
      LAS char* A = L + L_PHK + (lane >> 2) * PK + aw * 80 + (lane & 3) * 8;
      LAS char* Bv = L + L_V + (lane >> 2) * PV + aw * 48 + (lane & 3) * 16;
      v4u b0[4], b1[4], f[4][3];
#pragma unroll
      for (int s = 0; s < 4; ++s) {
        b0[s] = trd2(Bv + s * 32 * PV, 16 * PV); b1[s] = trd2(Bv + 8 + s * 32 * PV, 16 * PV);
#pragma unroll
        for (int T = 0; T < 3; ++T) f[s][T] = trd2(A + s * 32 * PK + T * 32, 16 * PK);
      }
#pragma unroll
      for (int s = 0; s < 4; ++s) {
#pragma unroll
        for (int T = 0; T < 3; ++T) { kv[0][T] = mfma32(f[s][T], b0[s], s ? kv[0][T] : f32x4{0, 0, 0, 0}); kv[1][T] = mfma32(f[s][T], b1[s], s ? kv[1][T] : f32x4{0, 0, 0, 0}); }
      }
      const unsigned d = WS_KVP + ((cg * 4 + aw) * 16 + panel) * KVB;
      const unsigned d0 = tok < 12 ? d + (g * 12 + tok) * 16 : OOB, d1 = tok < 9 ? d + (48 + g * 9 + tok) * 16 : OOB;
      st16(c.rw, d0, pk8(kv[0][0], kv[0][1]));
      st16(c.rw, d1, pk8(kv[1][0], kv[1][1]));
      st16(c.rw, (lane < 32 ? d0 : OOB) + 84 * 16, pk8(kv[0][2], kv[1][2]));
      SB();
      st4(c.rw, sel(lane < 8, MB_K + ((cg * 4 + aw) * 8 + lane) * 128 + panel * 4, OOB), c.tag);
    }
    if (!KV) {
      WAITV(0);
      arrive_flag(C_Q, 3, c.rw, sel(lane < 4, MB_Q + (((cg - 8) * 4 + lane) * 8 + (panel >> 1)) * 128 + (panel & 1) * 4, OOB), c.tag);
    }
    {
      const unsigned kf = MB_K + (head * 8 + tb) * 128;
      v16u fv; v4u pu[3];
      wait_flags<1>(p.ws, c.rw, qf, 0, c.tag);
      asm volatile("buffer_load_dwordx4 %0, %3, %4, 0 offen sc1\n\tbuffer_load_dwordx4 %1, %3, %4, 0 offen offset:1024 sc1\n\tbuffer_load_dwordx4 %2, %3, %4, 0 offen offset:2048 sc1"
                   : "=&v"(pu[0]), "=&v"(pu[1]), "=&v"(pu[2]) : "v"(qo), "s"(c.rw) : "memory");
      asm volatile("s_load_dwordx16 %0, %1, %2 glc" : "=&s"(fv) : "s"(p.ws), "s"(kf) : "memory");
      const int ci = lane & 31, sj = (tok & 3) + 4 * (tok >> 3);
      unsigned ko = sel(ci < 27, WS_KVP + (head * 16 + (lane >> 5) * 8) * KVB + (aw * 27 + ci) * 16, OOB);
      int kw = L_KV + (ci < 27 ? aw * 27 + ci : 108) * 16 + (lane >> 5) * 8;
      const unsigned sv = (g & 1) == ((tok >> 2) & 1) ? 0x3f80u << (sj & 1) * 16 : 0u;
      v4u sl = {sj >> 1 == 0 ? sv : 0u, sj >> 1 == 1 ? sv : 0u, sj >> 1 == 2 ? sv : 0u, sj >> 1 == 3 ? sv : 0u};
      const int c0 = tok < 12 ? tok : 0, c1 = tok < 8 ? tok : 8;
      int r0 = L_KV + (g * 12 + c0) * 16, r1 = L_KV + (48 + g * 9 + c1) * 16, r2 = L_KV + (84 + c0) * 16, r3 = L_KV + (84 + c1) * 16 + 8;
      const unsigned ob = WS_AFR + (tb * 16 + aw * 4) * NKS * 1024;
      unsigned oa = ob + ((lane >> 5) * NKS + 54 + (head >> 1)) * 1024 + (head & 1) * 512 + ci * 16;
      unsigned oc = ob + (((g & 1) * 2 + (g >> 1)) * NKS + 70 + (head >> 3)) * 1024 + ((head >> 1) & 3) * 256 + tok * 16 + (head & 1) * 8;
      unsigned of = sel(lane < 8, MB_O + ((tb * 4 + aw) * 8 + lane) * 256 + head * 4, OOB);
      asm volatile("" : "+v"(ko), "+v"(kw), "+v"(sl), "+v"(r0), "+v"(r1), "+v"(r2), "+v"(r3), "+v"(oa), "+v"(oc), "+v"(of));
      v16u ka, kb; unsigned t0, t1, it;
      asm volatile("" : "={v[224:239]}"(ka), "={v[240:255]}"(kb));
      asm volatile("s_waitcnt lgkmcnt(0)" : "+s"(fv)::"memory");
      const bool late = differs<16>(fv, c.tag);
      if (!late) { K_ISSUE(); WAITV(8); }
      else WAITV(0);
      asm volatile("" : "+v"(pu[0]), "+v"(pu[1]), "+v"(pu[2]));
      asm volatile("s_load_dwordx16 %0, %1, %2 glc" : "=&s"(fv) : "s"(p.ws), "s"(kf) : "memory");
      unsigned wv[20]; f32x2 u[10], e[10];
      const unsigned kz = pu[2].z;
      const float k = __builtin_bit_cast(float, kz);
#pragma unroll
      for (int i = 0; i < 10; ++i) { const unsigned x = pu[i >> 2][i & 3]; u[i] = f32x2{(float)(x & 0xffff), (float)(x >> 16)} * f32x2{k, k}; e[i] = u[i] - f32x2{65535.f * k, 65535.f * k}; }
      soft20(e, wv);
      asm volatile("" : "+v"(wv[0]), "+v"(wv[1]), "+v"(wv[2]), "+v"(wv[3]), "+v"(wv[4]), "+v"(wv[5]), "+v"(wv[6]), "+v"(wv[7]), "+v"(wv[8]), "+v"(wv[9]));
      asm volatile("s_waitcnt lgkmcnt(0)" : "+s"(fv)::"memory");
      if (late) {
        if (differs<16>(fv, c.tag)) wait_flags<16>(p.ws, c.rw, kf, 0, c.tag);
        K_ISSUE();
      }
      SB();
#pragma unroll
      for (int i = 0; i < 10; ++i) e[i] = -u[i];
      soft20(e, wv + 10);
      v4u qr[4];
#pragma unroll
      for (int i = 0; i < 4; ++i) {
        swap32(wv[i], wv[8 + i]); swap32(wv[4 + i], wv[12 + i]); swap16(wv[i], wv[4 + i]); swap16(wv[8 + i], wv[12 + i]);
        qr[i] = g == i ? v4u{wv[16], wv[17], wv[18], wv[19]} : v4u{0, 0, 0, 0};
      }
      asm volatile("" : "+v"(qr[0]), "+v"(qr[1]), "+v"(qr[2]), "+v"(qr[3]));
      SB();
      K_GET();
      f32x4 sa = mfma32(sl, sub4(ka, 0), f32x4{0, 0, 0, 0}), sb = mfma32(sl, sub4(kb, 0), f32x4{0, 0, 0, 0});
#pragma unroll
      for (int i = 1; i < 4; ++i) { sa = mfma32(sl, sub4(ka, i), sa); sb = mfma32(sl, sub4(kb, i), sb); }
      *(LAS v2u*)(L + kw) = pk4(sa + sb);
      arrive(C_K);
      await(L, C_K, 4);
      const v4u A00 = *(LAS v4u*)(L + r0), A01 = *(LAS v4u*)(L + r1);
      const v2u x0 = *(LAS v2u*)(L + r2), x1 = *(LAS v2u*)(L + r2 + 192), y0 = *(LAS v2u*)(L + r3), y1 = *(LAS v2u*)(L + r3 + 192);
      const v4u A10 = {x0.x, x0.y, x1.x, x1.y}, A11 = {y0.x, y0.y, y1.x, y1.y};
      f32x4 o0[4], o1[4];
#pragma unroll
      for (int i = 0; i < 4; ++i) { const v4u qm = {wv[4 * i], wv[4 * i + 1], wv[4 * i + 2], wv[4 * i + 3]}; o0[i] = mfma32(A00, qm, f32x4{0, 0, 0, 0}); o1[i] = mfma32(A01, qm, f32x4{0, 0, 0, 0}); }
#pragma unroll
      for (int i = 0; i < 4; ++i) { o0[i] = mfma32(A10, qr[i], o0[i]); o1[i] = mfma32(A11, qr[i], o1[i]); }
      v2u rm[2];
#pragma unroll
      for (int i = 0; i < 2; ++i) {
        v4u a0 = __builtin_bit_cast(v4u, o0[2 * i]), b0 = __builtin_bit_cast(v4u, o0[2 * i + 1]), a1 = __builtin_bit_cast(v4u, o1[2 * i]), b1 = __builtin_bit_cast(v4u, o1[2 * i + 1]);
#pragma unroll
        for (int j = 0; j < 4; ++j) { unsigned x = a1[j], y = b1[j]; swap32(x, y); a1[j] = x; b1[j] = y; x = a0[j]; y = b0[j]; swap32(x, y); a0[j] = x; b0[j] = y; }
        const float rd = __builtin_amdgcn_rcpf(__builtin_bit_cast(float, b1.x) + 1e-6f);
        st16(c.rw, oa + i * 2 * NKS * 1024, pk8(__builtin_bit_cast(f32x4, a0) * rd, __builtin_bit_cast(f32x4, a1) * rd));
        rm[i] = pk4(__builtin_bit_cast(f32x4, b0) * rd);
      }
      unsigned m0 = rm[0].x, m1 = rm[0].y, n0 = rm[1].x, n1 = rm[1].y;
      swap16(m0, n0); swap16(m1, n1);
      st8(c.rw, oc, v2u{m0, m1});
      SB();
      st4(c.rw, of, c.tag);
    }
#pragma unroll
    for (int j = 0; j < 5; ++j) wpre[4][j] = ld16(c.rw, wt + (j * NKS + 70 + aw) * 1024);
    SB();
    {
      v16u of[4]; v4u og[4]; unsigned t0, t1, it;
      const unsigned fo = afr + (54 + 4 * aw) * 1024, ro = afr + (70 + aw) * 1024;
      unsigned so[4];
#pragma unroll
      for (int n = 0; n < 4; ++n) so[n] = ((aw + 1 + n) & 3) * NKS * 1024;
      __builtin_amdgcn_s_setprio(PB1);
      wait_flags<8>(p.ws, c.rw, MB_O + (rt * 8 + ct) * 256 + aw * 32, 0, c.tag);
      if (BW >= 0) await(L, C_A + 4 * BW, 8);
      __builtin_amdgcn_s_waitcnt(0x0f70);
      SB();
      O_ISSUE0(); O_ISSUE1(); O_ISSUE2(); O_ISSUE3();
      f32x4 acc[2][5], t[5];
      unsigned cb, cx;
#pragma unroll
      for (int n = 0; n < 4; ++n) {
        if (n == 0) O_GET0(); else if (n == 1) O_GET1(); else if (n == 2) O_GET2(); else if (n == 3) O_GET3();
#pragma unroll
        for (int k = 0; k < 5; ++k) {
#pragma unroll
          for (int j = 0; j < 5; ++j) {
            acc[n & 1][j] = mfma32(wpre[k][j], k < 4 ? sub4(of[n], k) : og[n], k ? acc[n & 1][j] : f32x4{0, 0, 0, 0});
            if (n && k == 0) { *(LAS f32x4*)(L + L_XB + (((aw + n) & 3) * 3 + n - 1) * 5120 + j * 1024 + lane * 16) = acc[~n & 1][j]; SB(); }
          }
          if (n == 3 && k == 0) arrive(C_B);
          if (n == 3 && k == 1) { cb = peekv(L, C_B); cx = peekv(L, C_X + 4 * aw); }
          if (n == 3 && k == 2) {
            if (__builtin_amdgcn_readfirstlane(cb) < 4) await(L, C_B, 4);
            if (__builtin_amdgcn_readfirstlane(cx) < 1) await(L, C_X + 4 * aw, 1);
            asm volatile("" ::: "memory");
#pragma unroll
            for (int j = 0; j < 5; ++j) {
              LAS char* q = L + L_XB + aw * 15360 + j * 1024 + lane * 16;
              t[j] = (*(LAS f32x4*)q + *(LAS f32x4*)(q + 5120)) + (*(LAS f32x4*)(q + 10240) + *(LAS f32x4*)(L + ((aw + 3) & 3) * AW + j * 1024 + lane * 16));
            }
          } else if (n < 3 || k < 2) SB();
        }
      }
      asm volatile("hk_end1:");
      const int ln = lane_id();
      char* po = (char*)p.out + (size_t)(rt * 64 + aw * 16) * (D * 2) + ct * 160;
#pragma unroll
      for (int j = 0; j < 4; j += 2) {
        const v2u x = pk4(acc[1][j] + t[j]), y = pk4(acc[1][j + 1] + t[j + 1]);
        unsigned x0 = x.x, x1 = x.y, y0 = y.x, y1 = y.y;
        swap16(x0, y0); swap16(x1, y1);
        *(v4u*)(po + j * 32 + ((ln & 15) * (D * 2) + (ln & 16) * 2 + (ln >> 5) * 16)) = v4u{x0, x1, y0, y1};
      }
      *(v2u*)(po + 128 + ((ln & 15) * (D * 2) + (ln >> 4) * 8)) = pk4(acc[1][4] + t[4]);
    }
  }
}
__global__ __launch_bounds__(512, 2) void fk(P p) {
  const int w = __builtin_amdgcn_readfirstlane(threadIdx.x >> 6);
  unsigned cw[4];
  {
    u64 pc; unsigned n;
    asm volatile("s_getpc_b64 %0\nhk_beg:\ts_mov_b32 %1, hk_end0-hk_beg\n\ts_max_i32 %1, %1, hk_end1-hk_beg" : "=s"(pc), "=s"(n)::"scc");
    pc += __builtin_elementwise_min(((blockIdx.x >> 3) * 8 + w) * 256, n - 192);
    asm volatile("s_load_dword %0, %4, 0\n\ts_load_dword %1, %4, 64\n\ts_load_dword %2, %4, 128\n\ts_load_dword %3, %4, 192" : "=&s"(cw[0]), "=&s"(cw[1]), "=&s"(cw[2]), "=&s"(cw[3]) : "s"(pc) : "memory");
  }
  if (w < 4) { run<false>(p, w, cw); __builtin_amdgcn_endpgm(); }
  run<true>(p, w, cw);
}
}

extern "C" void kernel_launch(void* const* d_in, const int* in_sizes, int n_in, void* d_out, int out_size, void* d_ws, size_t ws_size, hipStream_t stream) {
  using namespace hk;
  P p;
  p.x = (const u16*)d_in[0]; p.wqkv = (const u16*)d_in[1]; p.wphi = (const u16*)d_in[2]; p.wo = (const u16*)d_in[3];
  p.w1 = (const u16*)d_in[5]; p.b1 = (const u16*)d_in[6]; p.w2 = (const u16*)d_in[7]; p.b2 = (const u16*)d_in[8];
  p.out = (u16*)d_out; p.ws = (char*)d_ws;
  (void)hipFuncSetAttribute((const void*)fk, hipFuncAttributeMaxDynamicSharedMemorySize, L_BYTES);
  fk<<<256, 512, L_BYTES, stream>>>(p);
}
```

```cpp
#include <hip/hip_runtime.h>
#include <hip/hip_bf16.h>
#include <stdint.h>

#define GAS __attribute__((address_space(1)))
#define LAS __attribute__((address_space(3)))
#define DI __device__ __forceinline__
typedef unsigned short u16;
typedef unsigned long long u64;
typedef unsigned v4u __attribute__((ext_vector_type(4)));
typedef unsigned v2u __attribute__((ext_vector_type(2)));
typedef float f32x4 __attribute__((ext_vector_type(4)));
typedef unsigned v8u __attribute__((ext_vector_type(8)));
typedef unsigned v16u __attribute__((ext_vector_type(16)));
typedef float f32x8 __attribute__((ext_vector_type(8)));
typedef float f32x16 __attribute__((ext_vector_type(16)));
typedef float f32x32 __attribute__((ext_vector_type(32)));
typedef float f32x2 __attribute__((ext_vector_type(2)));
typedef short s16x4 __attribute__((ext_vector_type(4)));
typedef short s16x8 __attribute__((ext_vector_type(8)));
typedef __bf16 bf16x2 __attribute__((ext_vector_type(2)));

#define OP 1
#define BP(s, v) ((unsigned)__builtin_amdgcn_ds_bpermute(s, (int)(v)))
#define HM 16
#define OB 0
#define OAL 1
#define OA 0
namespace hk {
constexpr int S = 2048, D = 640, F = 1728, NKS = 74;
constexpr int NCOL = 3 * D + F;
constexpr unsigned WS_EPOCH = 0;
constexpr unsigned WS_FLAG = 4096;
constexpr unsigned FL_PT = WS_FLAG;
constexpr unsigned MB_H = WS_FLAG + 4096, MB_X = MB_H + 64, MB_O = MB_H + 128;
constexpr unsigned MB_K = MB_H + 65536, MB_Q = MB_K + 64;
constexpr unsigned WS_RS = MB_K + 32768;
constexpr unsigned WS_CS = WS_RS + S * 4;
constexpr unsigned WS_AFR = 262144;
constexpr unsigned WS_WT = WS_AFR + 128 * NKS * 1024;
constexpr unsigned WS_PHQ = WS_WT + 40 * NKS * 1024;
constexpr unsigned KVB = 1792;
constexpr unsigned WS_KVP = WS_PHQ + 32 * 32 * 3072;
constexpr unsigned WS_XCH = WS_KVP + 32 * 16 * KVB;
constexpr unsigned WS_END = WS_XCH + 256 * 20480;
static_assert(WS_CS + 4 * NCOL * 4 <= WS_AFR, "ws");
constexpr int XB = 16384, WR = 1056, BUF = XB + 32 * WR;
constexpr int L_WPHI = 3 * BUF, L_ZERO = L_WPHI + 3328, L_CS = L_ZERO + 1024, L_B1 = L_CS + 1024, L_CP = L_B1 + 1024, L_RS = L_CP, L_ST = L_CP + 1280, L_CNT = L_ST + 32, L_KV = L_CNT + 96, L_BYTES = L_KV + 3072 + 1024;
constexpr int PK = 352, PV = 224;
constexpr int L_V = 3 * BUF - 128 * PV - 64, L_PHK = L_V - 128 * PK - 64;
constexpr int AW = 15360, L_RES = 4 * AW, L_XB = L_PHK;
constexpr int C_H = L_CNT, C_K = L_CNT + 4, C_V = L_CNT + 8, C_Q = L_CNT + 12, C_B = L_CNT + 52, C_X = L_CNT + 24, C_A = L_CNT + 40, C_R = L_CNT + 16, C_D = L_CNT + 20, C_S = L_CNT + 64;
constexpr unsigned OOB = 0xf0000000u;
static_assert(L_PHK >= BUF && L_RES >= BUF && L_RES + 12288 <= L_PHK && L_XB + 61440 <= 3 * BUF && L_BYTES <= 163840, "lds");

DI f32x4 mfma32(v4u a, v4u b, f32x4 c) { return __builtin_amdgcn_mfma_f32_16x16x32_bf16(__builtin_bit_cast(s16x8, a), __builtin_bit_cast(s16x8, b), c, 0, 0, 0); }
DI f32x4 mfma16(v2u a, v2u b, f32x4 c) { return __builtin_amdgcn_mfma_f32_16x16x16bf16_1k(__builtin_bit_cast(s16x4, a), __builtin_bit_cast(s16x4, b), c, 0, 0, 0); }
DI v2u trd(LAS char* p) { return __builtin_bit_cast(v2u, __builtin_amdgcn_ds_read_tr16_b64_v4i16((LAS s16x4*)p)); }
DI v4u trd2(LAS char* p, int d) { v2u a = trd(p), b = trd(p + d); return v4u{a.x, a.y, b.x, b.y}; }
DI unsigned pk(float a, float b) { f32x2 v = {a, b}; return __builtin_bit_cast(unsigned, __builtin_convertvector(v, bf16x2)); }
DI v2u pk4(f32x4 a) { return v2u{pk(a[0], a[1]), pk(a[2], a[3])}; }
DI v4u pk8(f32x4 a, f32x4 b) { return v4u{pk(a[0], a[1]), pk(a[2], a[3]), pk(b[0], b[1]), pk(b[2], b[3])}; }
DI float lo(unsigned u) { return __builtin_bit_cast(float, u << 16); }
DI float hi(unsigned u) { return __builtin_bit_cast(float, u & 0xffff0000u); }
DI float rb(float a) { return lo(pk(a, a)); }
DI void glds(const void* g, unsigned dst) { asm volatile("s_mov_b32 m0, %1\n\ts_nop 0\n\tglobal_load_lds_dwordx4 %0, off" ::"v"(g), "s"(dst) : "memory"); }
DI void gldso(const void* base, unsigned off, unsigned dst) { asm volatile("s_mov_b32 m0, %2\n\ts_nop 0\n\tglobal_load_lds_dwordx4 %0, %1" ::"v"(off), "s"(base), "s"(dst) : "memory"); }
DI float dot2(unsigned a, unsigned b, float c) { return __builtin_amdgcn_fdot2_f32_bf16(__builtin_bit_cast(bf16x2, a), __builtin_bit_cast(bf16x2, b), c, false); }
DI int lane_id() { int l; asm volatile("v_mbcnt_lo_u32_b32 %0, -1, 0\n\tv_mbcnt_hi_u32_b32 %0, -1, %0" : "=v"(l)); return l; }
#define BAR() __builtin_amdgcn_s_barrier()
#define WAITV(n) asm volatile("s_waitcnt vmcnt(" #n ")" ::: "memory")
#define WAITL0() asm volatile("s_waitcnt lgkmcnt(0)" ::: "memory")
#define SB() __builtin_amdgcn_sched_barrier(0)
typedef __amdgpu_buffer_rsrc_t rsrc_t;
DI v4u ld16(rsrc_t r, unsigned off) { return __builtin_amdgcn_raw_buffer_load_b128(r, off, 0, 16); }
DI v4u ld16s(rsrc_t r, unsigned off, unsigned so) { return __builtin_amdgcn_raw_buffer_load_b128(r, off, so, 16); }
DI v2u ld8(rsrc_t r, unsigned off) { return __builtin_amdgcn_raw_buffer_load_b64(r, off, 0, 16); }
DI unsigned ld4(rsrc_t r, unsigned off) { return __builtin_amdgcn_raw_buffer_load_b32(r, off, 0, 16); }
DI void st16(rsrc_t r, unsigned off, v4u v) { __builtin_amdgcn_raw_buffer_store_b128(v, r, off, 0, 16); }
DI void st8(rsrc_t r, unsigned off, v2u v) { __builtin_amdgcn_raw_buffer_store_b64(v, r, off, 0, 16); }
DI void st4(rsrc_t r, unsigned off, unsigned v) { __builtin_amdgcn_raw_buffer_store_b32(v, r, off, 0, 16); }
constexpr unsigned SPINS = 1u << 16;
#define INF 12
#define FLEAD 8
#define NWT 250
#define LDJ 1
#define XD 8
#define HPAR 0
#define NP1 6
#define NP0 0
#define STL 1
#define CWV 0
#define ROT 0, 1, 2, 3
#define PUBS 0
#define SENS 0
#define FLS ((PUBS > SENS ? PUBS : SENS) + 1)
#define QH 0
#define KH 0
#define Q1 2
#define Q2 4
#define XO 1
#define HQ 1
#define HW 1
#define WS0 2
#define WST 1
#define TP 9
#define TA 23
#define XFLIP 0
#define SC1 0
#define SC2 0
#define SC3 0
#define SC4 1
#define SC5 1
#define CL 1
#define OE 1
#define PAQ 1
#define PAK 1
#define PAV 1
#define TQ 0
#define TK 0
#define LT 3
#define QE 0
#define PB0 2
#define PA0 1
#define PB1 0
#define BW -1
#define XS 255
#define XC 270
#define RPOS 0
#define RTA(ct) 0
#define RTB(ct) (ct)
#define XM 12
#define RA 8
#define K1MAP 1
#define K2MAP 2
DI void wait_tail(rsrc_t r, unsigned off, unsigned tag) {
#pragma unroll 1
  for (unsigned i = 0; i < SPINS; ++i) {
    asm volatile("" ::: "memory");
    if (__builtin_amdgcn_ballot_w64(ld4(r, off) != tag) == 0) break;
  }
}
template <int N, class V> DI unsigned differs(const V& v, unsigned tag) {
  u64 a = ~0ull, o = 0;
#pragma unroll
  for (int i = 0; i < N; i += 2) { const u64 x = (u64)v[i + 1] << 32 | v[i]; a &= x; o |= x; }
  unsigned l = (unsigned)a & (unsigned)(a >> 32), h = (unsigned)o | (unsigned)(o >> 32);
  asm("" : "+s"(l), "+s"(h));
  return (l ^ tag) | (h ^ tag);
}
template <int N> DI void wait_flags(const char* ws, rsrc_t r, unsigned off, unsigned off1, unsigned tag) {
  unsigned d = 1;
#pragma unroll 1
  for (unsigned i = 0; i < 4096 && d; ++i) {
    if (N == 1) { unsigned v; asm volatile("s_load_dword %0, %1, %2 glc\n\ts_waitcnt lgkmcnt(0)" : "=s"(v) : "s"(ws), "s"(off) : "memory"); d = v ^ tag; }
    else if (N == 8) { v8u v; asm volatile("s_load_dwordx8 %0, %1, %2 glc\n\ts_waitcnt lgkmcnt(0)" : "=s"(v) : "s"(ws), "s"(off) : "memory"); d = differs<8>(v, tag); }
    else if (N == 16) { v16u v; asm volatile("s_load_dwordx16 %0, %1, %2 glc\n\ts_waitcnt lgkmcnt(0)" : "=s"(v) : "s"(ws), "s"(off) : "memory"); d = differs<16>(v, tag); }
    else { v16u v; unsigned u; asm volatile("s_load_dwordx16 %0, %2, %3 glc\n\ts_load_dword %1, %2, %4 glc\n\ts_waitcnt lgkmcnt(0)" : "=&s"(v), "=&s"(u) : "s"(ws), "s"(off), "s"(off1) : "memory"); d = differs<16>(v, tag) | (u ^ tag); }
  }
  if (d) { const unsigned l = threadIdx.x & 63; wait_tail(r, N == 17 && l == 16 ? off1 : off + (l & (N == 17 ? 15 : N - 1)) * 4, tag); }
}
DI void arrive(int c) { asm volatile("s_mov_b64 exec, 1\n\tds_add_u32 %0, %1\n\ts_mov_b64 exec, -1" ::"v"(c), "v"(1u) : "memory"); }
template <bool W = false> DI void arrive_flag(int c, unsigned last, rsrc_t r, unsigned off, unsigned tag) {
  unsigned t, u;
  if (W) asm volatile("s_mov_b64 exec, 1\n\tds_add_rtn_u32 %0, %2, %3\n\ts_mov_b64 exec, -1\n\ts_waitcnt lgkmcnt(0)\n\tv_readfirstlane_b32 %1, %0\n\ts_cmp_eq_u32 %1, %4\n\ts_cbranch_scc1 1f\n\ts_waitcnt vmcnt(0)\n\ts_branch 2f\n1:\tbuffer_store_dword %5, %6, %7, 0 offen sc1\n\ts_waitcnt vmcnt(1)\n2:"
               : "=&v"(t), "=&s"(u) : "v"(c), "v"(1u), "n"(last), "v"(tag), "v"(off), "s"(r) : "memory", "scc");
  else
  asm volatile("s_mov_b64 exec, 1\n\tds_add_rtn_u32 %0, %2, %3\n\ts_mov_b64 exec, -1\n\ts_waitcnt lgkmcnt(0)\n\tv_readfirstlane_b32 %1, %0\n\ts_cmp_lg_u32 %1, %4\n\ts_cbranch_scc1 1f\n\tbuffer_store_dword %5, %6, %7, 0 offen sc1\n1:"
               : "=&v"(t), "=&s"(u) : "v"(c), "v"(1u), "n"(last), "v"(tag), "v"(off), "s"(r) : "memory", "scc");
}
DI unsigned peekv(LAS char* L, int c) { return __hip_atomic_load((LAS unsigned*)(L + c), __ATOMIC_RELAXED, __HIP_MEMORY_SCOPE_WORKGROUP); }
DI unsigned peek(LAS char* L, int c) { return __builtin_amdgcn_readfirstlane(peekv(L, c)); }
DI void await(LAS char* L, int c, unsigned n) {
  #pragma unroll 1
  for (unsigned i = 0; i < (1u << 22) && peek(L, c) < n; ++i) __builtin_amdgcn_s_sleep(1);
  asm volatile("" ::: "memory");
}
DI void recheck(unsigned v, int c, unsigned n) {
  unsigned t, i;
  asm volatile("s_mov_b32 %2, 0x400000\n1:\tv_readfirstlane_b32 %1, %0\n\ts_cmp_ge_u32 %1, %4\n\ts_cbranch_scc1 2f\n\ts_sleep 1\n\tv_mov_b32 %0, %3\n\tds_read_b32 %0, %0\n\ts_sub_u32 %2, %2, 1\n\ts_waitcnt lgkmcnt(0)\n\ts_cbranch_scc0 1b\n2:"
               : "+v"(v), "=&s"(t), "=&s"(i) : "n"(c), "n"(n) : "memory", "scc");
}
DI void recheckv(unsigned v, int c, unsigned n) {
  unsigned t, i;
  asm volatile("s_mov_b32 %2, 0x400000\n1:\tv_readfirstlane_b32 %1, %0\n\ts_cmp_ge_u32 %1, %4\n\ts_cbranch_scc1 2f\n\ts_sleep 1\n\tv_mov_b32 %0, %3\n\tds_read_b32 %0, %0\n\ts_sub_u32 %2, %2, 1\n\ts_waitcnt lgkmcnt(0)\n\ts_cbranch_scc0 1b\n2:"
               : "+v"(v), "=&s"(t), "=&s"(i) : "s"(c), "n"(n) : "memory", "scc");
}
DI unsigned max3u(unsigned a, unsigned b, unsigned c) { return __builtin_elementwise_max(__builtin_elementwise_max(a, b), c); }
DI unsigned sel(bool c, unsigned a, unsigned b) { return c ? a : b; }
DI v4u sub4(const v16u& v, int i) { return v4u{v[4 * i], v[4 * i + 1], v[4 * i + 2], v[4 * i + 3]}; }
#define Q_LOADS "buffer_load_dwordx4 %0, %[qv], %[rw], %[qo] offen sc1\n\tbuffer_load_dwordx4 %1, %[qv], %[rw], %[qo] offen offset:1024 sc1\n\tbuffer_load_dwordx4 %2, %[qv], %[rw], %[qo] offen offset:2048 sc1"
#define K_ISSUE() asm volatile( \
        "buffer_load_dwordx4 v[224:227], %[ko], %[rw], 0 offen sc1\n\t" "buffer_load_dwordx4 v[228:231], %[ko], %[rw], 0 offen offset:1792 sc1\n\t"  \
        "buffer_load_dwordx4 v[232:235], %[ko], %[rw], 0 offen offset:3584 sc1\n\t" "buffer_load_dwordx4 v[236:239], %[ko], %[rw], %[s1] offen sc1\n\t"  \
        "buffer_load_dwordx4 v[240:243], %[ko], %[rw], %[s1] offen offset:1792 sc1\n\t" "buffer_load_dwordx4 v[244:247], %[ko], %[rw], %[s1] offen offset:3584 sc1\n\t"  \
        "buffer_load_dwordx4 v[248:251], %[ko], %[rw], %[s2] offen sc1\n\t" "buffer_load_dwordx4 v[252:255], %[ko], %[rw], %[s2] offen offset:1792 sc1\n\t" "" \
        : "+{v[224:239]}"(ka), "+{v[240:255]}"(kb) : [ko] "v"(ko), [rw] "s"(c.rw), [s1] "s"(3 * KVB), [s2] "s"(6 * KVB) : "memory")
#define STR2(x) #x
#define STR(x) STR2(x)
#define XOS STR(XO)
#define K_GET() asm volatile( \
        "s_mov_b32 %[it], %[sp]\n\t" "s_waitcnt vmcnt(0)\n\t" "1:\tv_max3_u32 %[t0], v224, v225, v226\n\t" "v_max3_u32 %[t1], v227, v228, v229\n\t" "v_max3_u32 %[t0], %[t0], v230, v231\n\t"  \
        "v_max3_u32 %[t1], %[t1], v232, v233\n\t" "v_max3_u32 %[t0], %[t0], v234, v235\n\t" "v_max3_u32 %[t1], %[t1], v236, v237\n\t" "v_max3_u32 %[t0], %[t0], v238, v239\n\t"  \
        "v_max3_u32 %[t1], %[t1], v240, v241\n\t" "v_max3_u32 %[t0], %[t0], v242, v243\n\t" "v_max3_u32 %[t1], %[t1], v244, v245\n\t" "v_max3_u32 %[t0], %[t0], v246, v247\n\t"  \
        "v_max3_u32 %[t1], %[t1], v248, v249\n\t" "v_max3_u32 %[t0], %[t0], v250, v251\n\t" "v_max3_u32 %[t1], %[t1], v252, v253\n\t" "v_max3_u32 %[t0], %[t0], v254, v255\n\t"  \
        "v_max_u32 %[t0], %[t0], %[t1]\n\t" "v_cmp_eq_u32 vcc, -1, %[t0]\n\t" "s_cbranch_vccz 2f\n\t" "buffer_load_dwordx4 v[224:227], %[ko], %[rw], 0 offen sc1\n\t"  \
        "buffer_load_dwordx4 v[228:231], %[ko], %[rw], 0 offen offset:1792 sc1\n\t" "buffer_load_dwordx4 v[232:235], %[ko], %[rw], 0 offen offset:3584 sc1\n\t"  \
        "buffer_load_dwordx4 v[236:239], %[ko], %[rw], %[s1] offen sc1\n\t" "buffer_load_dwordx4 v[240:243], %[ko], %[rw], %[s1] offen offset:1792 sc1\n\t"  \
        "buffer_load_dwordx4 v[244:247], %[ko], %[rw], %[s1] offen offset:3584 sc1\n\t" "buffer_load_dwordx4 v[248:251], %[ko], %[rw], %[s2] offen sc1\n\t"  \
        "buffer_load_dwordx4 v[252:255], %[ko], %[rw], %[s2] offen offset:1792 sc1\n\t" "s_waitcnt vmcnt(0)\n\t" "s_sub_u32 %[it], %[it], 1\n\t" "s_cbranch_scc0 1b\n\t" "2:" \
        : "+{v[224:239]}"(ka), "+{v[240:255]}"(kb), [t0] "=&v"(t0), [t1] "=&v"(t1), [it] "=&s"(it) \
        : [ko] "v"(ko), [rw] "s"(c.rw), [s1] "s"(3 * KVB), [s2] "s"(6 * KVB), [sp] "n"(SPINS) : "memory", "vcc", "scc")
#define O_ISSUE0() asm volatile( \
        "buffer_load_dwordx4 v[176:179], %[fo], %[rw], %[so] offen sc1\n\t" "buffer_load_dwordx4 v[180:183], %[fo], %[rw], %[so] offen offset:1024 sc1\n\t"  \
        "buffer_load_dwordx4 v[184:187], %[fo], %[rw], %[so] offen offset:2048 sc1\n\t" "buffer_load_dwordx4 v[188:191], %[fo], %[rw], %[so] offen offset:3072 sc1\n\t"  \
        "buffer_load_dwordx4 v[192:195], %[ro], %[rw], %[so] offen sc1\n\t" "" \
        : "=&{v[176:179]}"(of[0][0]), "=&{v[180:183]}"(of[0][1]), "=&{v[184:187]}"(of[0][2]), "=&{v[188:191]}"(of[0][3]), "=&{v[192:195]}"(of[0][4]) : [fo] "v"(fo), [ro] "v"(ro), [rw] "s"(c.rw), [so] "s"(so[0]) : "memory")
#define O_WAIT0() asm volatile("s_waitcnt vmcnt(10+" XOS ")" : "+{v[176:179]}"(of[0][0]), "+{v[180:183]}"(of[0][1]), "+{v[184:187]}"(of[0][2]), "+{v[188:191]}"(of[0][3]), "+{v[192:195]}"(of[0][4]) :: "memory")
#define O_FIX0() asm volatile( \
        "s_mov_b32 %[it], %[sp]\n\t" "1:\tv_cmp_eq_u32 vcc, -1, %[t0]\n\t" "s_cbranch_vccz 2f\n\t" "buffer_load_dwordx4 v[176:179], %[fo], %[rw], %[so] offen sc1\n\t"  \
        "buffer_load_dwordx4 v[180:183], %[fo], %[rw], %[so] offen offset:1024 sc1\n\t" "buffer_load_dwordx4 v[184:187], %[fo], %[rw], %[so] offen offset:2048 sc1\n\t"  \
        "buffer_load_dwordx4 v[188:191], %[fo], %[rw], %[so] offen offset:3072 sc1\n\t" "buffer_load_dwordx4 v[192:195], %[ro], %[rw], %[so] offen sc1\n\t" "s_waitcnt vmcnt(0)\n\t"  \
        "v_max_u32 %[t0], v176, v177\n\t" "v_max3_u32 %[t0], %[t0], v178, v179\n\t" "v_max3_u32 %[t0], %[t0], v180, v181\n\t" "v_max3_u32 %[t0], %[t0], v182, v183\n\t"  \
        "v_max3_u32 %[t0], %[t0], v184, v185\n\t" "v_max3_u32 %[t0], %[t0], v186, v187\n\t" "v_max3_u32 %[t0], %[t0], v188, v189\n\t" "v_max3_u32 %[t0], %[t0], v190, v191\n\t"  \
        "v_max3_u32 %[t0], %[t0], v192, v193\n\t" "v_max3_u32 %[t0], %[t0], v194, v195\n\t" "s_sub_u32 %[it], %[it], 1\n\t" "s_cbranch_scc0 1b\n\t" "2:" \
        : "+{v[176:179]}"(of[0][0]), "+{v[180:183]}"(of[0][1]), "+{v[184:187]}"(of[0][2]), "+{v[188:191]}"(of[0][3]), "+{v[192:195]}"(of[0][4]), [t0] "+v"(t0), [it] "=&s"(it) \
        : [fo] "v"(fo), [ro] "v"(ro), [rw] "s"(c.rw), [so] "s"(so[0]), [sp] "n"(SPINS) : "memory", "vcc", "scc")
#define O_ISSUE1() asm volatile( \
        "buffer_load_dwordx4 v[196:199], %[fo], %[rw], %[so] offen sc1\n\t" "buffer_load_dwordx4 v[200:203], %[fo], %[rw], %[so] offen offset:1024 sc1\n\t"  \
        "buffer_load_dwordx4 v[204:207], %[fo], %[rw], %[so] offen offset:2048 sc1\n\t" "buffer_load_dwordx4 v[208:211], %[fo], %[rw], %[so] offen offset:3072 sc1\n\t"  \
        "buffer_load_dwordx4 v[212:215], %[ro], %[rw], %[so] offen sc1\n\t" "" \
        : "=&{v[196:199]}"(of[1][0]), "=&{v[200:203]}"(of[1][1]), "=&{v[204:207]}"(of[1][2]), "=&{v[208:211]}"(of[1][3]), "=&{v[212:215]}"(of[1][4]) : [fo] "v"(fo), [ro] "v"(ro), [rw] "s"(c.rw), [so] "s"(so[1]) : "memory")
#define O_WAIT1() asm volatile("s_waitcnt vmcnt(10+" XOS ")" : "+{v[196:199]}"(of[1][0]), "+{v[200:203]}"(of[1][1]), "+{v[204:207]}"(of[1][2]), "+{v[208:211]}"(of[1][3]), "+{v[212:215]}"(of[1][4]) :: "memory")
#define O_FIX1() asm volatile( \
        "s_mov_b32 %[it], %[sp]\n\t" "1:\tv_cmp_eq_u32 vcc, -1, %[t0]\n\t" "s_cbranch_vccz 2f\n\t" "buffer_load_dwordx4 v[196:199], %[fo], %[rw], %[so] offen sc1\n\t"  \
        "buffer_load_dwordx4 v[200:203], %[fo], %[rw], %[so] offen offset:1024 sc1\n\t" "buffer_load_dwordx4 v[204:207], %[fo], %[rw], %[so] offen offset:2048 sc1\n\t"  \
        "buffer_load_dwordx4 v[208:211], %[fo], %[rw], %[so] offen offset:3072 sc1\n\t" "buffer_load_dwordx4 v[212:215], %[ro], %[rw], %[so] offen sc1\n\t" "s_waitcnt vmcnt(0)\n\t"  \
        "v_max_u32 %[t0], v196, v197\n\t" "v_max3_u32 %[t0], %[t0], v198, v199\n\t" "v_max3_u32 %[t0], %[t0], v200, v201\n\t" "v_max3_u32 %[t0], %[t0], v202, v203\n\t"  \
        "v_max3_u32 %[t0], %[t0], v204, v205\n\t" "v_max3_u32 %[t0], %[t0], v206, v207\n\t" "v_max3_u32 %[t0], %[t0], v208, v209\n\t" "v_max3_u32 %[t0], %[t0], v210, v211\n\t"  \
        "v_max3_u32 %[t0], %[t0], v212, v213\n\t" "v_max3_u32 %[t0], %[t0], v214, v215\n\t" "s_sub_u32 %[it], %[it], 1\n\t" "s_cbranch_scc0 1b\n\t" "2:" \
        : "+{v[196:199]}"(of[1][0]), "+{v[200:203]}"(of[1][1]), "+{v[204:207]}"(of[1][2]), "+{v[208:211]}"(of[1][3]), "+{v[212:215]}"(of[1][4]), [t0] "+v"(t0), [it] "=&s"(it) \
        : [fo] "v"(fo), [ro] "v"(ro), [rw] "s"(c.rw), [so] "s"(so[1]), [sp] "n"(SPINS) : "memory", "vcc", "scc")
#define O_ISSUE2() asm volatile( \
        "buffer_load_dwordx4 v[216:219], %[fo], %[rw], %[so] offen sc1\n\t" "buffer_load_dwordx4 v[220:223], %[fo], %[rw], %[so] offen offset:1024 sc1\n\t"  \
        "buffer_load_dwordx4 v[224:227], %[fo], %[rw], %[so] offen offset:2048 sc1\n\t" "buffer_load_dwordx4 v[228:231], %[fo], %[rw], %[so] offen offset:3072 sc1\n\t"  \
        "buffer_load_dwordx4 v[232:235], %[ro], %[rw], %[so] offen sc1\n\t" "" \
        : "=&{v[216:219]}"(of[2][0]), "=&{v[220:223]}"(of[2][1]), "=&{v[224:227]}"(of[2][2]), "=&{v[228:231]}"(of[2][3]), "=&{v[232:235]}"(of[2][4]) : [fo] "v"(fo), [ro] "v"(ro), [rw] "s"(c.rw), [so] "s"(so[2]) : "memory")
#define O_WAIT2() asm volatile("s_waitcnt vmcnt(5+" XOS ")" : "+{v[216:219]}"(of[2][0]), "+{v[220:223]}"(of[2][1]), "+{v[224:227]}"(of[2][2]), "+{v[228:231]}"(of[2][3]), "+{v[232:235]}"(of[2][4]) :: "memory")
#define O_FIX2() asm volatile( \
        "s_mov_b32 %[it], %[sp]\n\t" "1:\tv_cmp_eq_u32 vcc, -1, %[t0]\n\t" "s_cbranch_vccz 2f\n\t" "buffer_load_dwordx4 v[216:219], %[fo], %[rw], %[so] offen sc1\n\t"  \
        "buffer_load_dwordx4 v[220:223], %[fo], %[rw], %[so] offen offset:1024 sc1\n\t" "buffer_load_dwordx4 v[224:227], %[fo], %[rw], %[so] offen offset:2048 sc1\n\t"  \
        "buffer_load_dwordx4 v[228:231], %[fo], %[rw], %[so] offen offset:3072 sc1\n\t" "buffer_load_dwordx4 v[232:235], %[ro], %[rw], %[so] offen sc1\n\t" "s_waitcnt vmcnt(0)\n\t"  \
        "v_max_u32 %[t0], v216, v217\n\t" "v_max3_u32 %[t0], %[t0], v218, v219\n\t" "v_max3_u32 %[t0], %[t0], v220, v221\n\t" "v_max3_u32 %[t0], %[t0], v222, v223\n\t"  \
        "v_max3_u32 %[t0], %[t0], v224, v225\n\t" "v_max3_u32 %[t0], %[t0], v226, v227\n\t" "v_max3_u32 %[t0], %[t0], v228, v229\n\t" "v_max3_u32 %[t0], %[t0], v230, v231\n\t"  \
        "v_max3_u32 %[t0], %[t0], v232, v233\n\t" "v_max3_u32 %[t0], %[t0], v234, v235\n\t" "s_sub_u32 %[it], %[it], 1\n\t" "s_cbranch_scc0 1b\n\t" "2:" \
        : "+{v[216:219]}"(of[2][0]), "+{v[220:223]}"(of[2][1]), "+{v[224:227]}"(of[2][2]), "+{v[228:231]}"(of[2][3]), "+{v[232:235]}"(of[2][4]), [t0] "+v"(t0), [it] "=&s"(it) \
        : [fo] "v"(fo), [ro] "v"(ro), [rw] "s"(c.rw), [so] "s"(so[2]), [sp] "n"(SPINS) : "memory", "vcc", "scc")
#define O_LD30() asm volatile("buffer_load_dwordx4 v[236:239], %[fo], %[rw], %[so] offen sc1" : "=&{v[236:239]}"(of[3][0]) : [fo] "v"(fo), [ro] "v"(ro), [rw] "s"(c.rw), [so] "s"(so[3]) : "memory")
#define O_LD31() asm volatile("buffer_load_dwordx4 v[240:243], %[fo], %[rw], %[so] offen offset:1024 sc1" : "=&{v[240:243]}"(of[3][1]) : [fo] "v"(fo), [ro] "v"(ro), [rw] "s"(c.rw), [so] "s"(so[3]) : "memory")
#define O_LD32() asm volatile("buffer_load_dwordx4 v[244:247], %[fo], %[rw], %[so] offen offset:2048 sc1" : "=&{v[244:247]}"(of[3][2]) : [fo] "v"(fo), [ro] "v"(ro), [rw] "s"(c.rw), [so] "s"(so[3]) : "memory")
#define O_LD33() asm volatile("buffer_load_dwordx4 v[248:251], %[fo], %[rw], %[so] offen offset:3072 sc1" : "=&{v[248:251]}"(of[3][3]) : [fo] "v"(fo), [ro] "v"(ro), [rw] "s"(c.rw), [so] "s"(so[3]) : "memory")
#define O_LD34() asm volatile("buffer_load_dwordx4 v[252:255], %[ro], %[rw], %[so] offen sc1" : "=&{v[252:255]}"(of[3][4]) : [fo] "v"(fo), [ro] "v"(ro), [rw] "s"(c.rw), [so] "s"(so[3]) : "memory")
#define O_WAIT3() asm volatile("s_waitcnt vmcnt(5)" : "+{v[236:239]}"(of[3][0]), "+{v[240:243]}"(of[3][1]), "+{v[244:247]}"(of[3][2]), "+{v[248:251]}"(of[3][3]), "+{v[252:255]}"(of[3][4]) :: "memory")
#define O_FIX3() asm volatile( \
        "s_mov_b32 %[it], %[sp]\n\t" "1:\tv_cmp_eq_u32 vcc, -1, %[t0]\n\t" "s_cbranch_vccz 2f\n\t" "buffer_load_dwordx4 v[236:239], %[fo], %[rw], %[so] offen sc1\n\t"  \
        "buffer_load_dwordx4 v[240:243], %[fo], %[rw], %[so] offen offset:1024 sc1\n\t" "buffer_load_dwordx4 v[244:247], %[fo], %[rw], %[so] offen offset:2048 sc1\n\t"  \
        "buffer_load_dwordx4 v[248:251], %[fo], %[rw], %[so] offen offset:3072 sc1\n\t" "buffer_load_dwordx4 v[252:255], %[ro], %[rw], %[so] offen sc1\n\t" "s_waitcnt vmcnt(0)\n\t"  \
        "v_max_u32 %[t0], v236, v237\n\t" "v_max3_u32 %[t0], %[t0], v238, v239\n\t" "v_max3_u32 %[t0], %[t0], v240, v241\n\t" "v_max3_u32 %[t0], %[t0], v242, v243\n\t"  \
        "v_max3_u32 %[t0], %[t0], v244, v245\n\t" "v_max3_u32 %[t0], %[t0], v246, v247\n\t" "v_max3_u32 %[t0], %[t0], v248, v249\n\t" "v_max3_u32 %[t0], %[t0], v250, v251\n\t"  \
        "v_max3_u32 %[t0], %[t0], v252, v253\n\t" "v_max3_u32 %[t0], %[t0], v254, v255\n\t" "s_sub_u32 %[it], %[it], 1\n\t" "s_cbranch_scc0 1b\n\t" "2:" \
        : "+{v[236:239]}"(of[3][0]), "+{v[240:243]}"(of[3][1]), "+{v[244:247]}"(of[3][2]), "+{v[248:251]}"(of[3][3]), "+{v[252:255]}"(of[3][4]), [t0] "+v"(t0), [it] "=&s"(it) \
        : [fo] "v"(fo), [ro] "v"(ro), [rw] "s"(c.rw), [so] "s"(so[3]), [sp] "n"(SPINS) : "memory", "vcc", "scc")
template <int CTRL, int RM> DI float dpp_add(float v) { return v + __builtin_bit_cast(float, __builtin_amdgcn_update_dpp(0, __builtin_bit_cast(int, v), CTRL, RM, 0xf, false)); }
DI float wave_sum(float v) {
  v = dpp_add<0x111, 0xf>(v); v = dpp_add<0x112, 0xf>(v); v = dpp_add<0x114, 0xf>(v); v = dpp_add<0x118, 0xf>(v);
  v = dpp_add<0x142, 0xa>(v); v = dpp_add<0x143, 0xc>(v);
  return __builtin_bit_cast(float, __builtin_amdgcn_readlane(__builtin_bit_cast(int, v), 63));
}

struct P {
  const u16 *x, *wqkv, *wphi, *wo, *w1, *b1, *w2, *b2;
  u16* out;
  char* ws;
};

DI void range20(const f32x4 (&a)[5], float& mx, float& mn) {
  float m0 = __builtin_fmaxf(a[0][0], a[0][1]), m1 = __builtin_fmaxf(a[0][2], a[0][3]), n0 = __builtin_fminf(a[0][0], a[0][1]), n1 = __builtin_fminf(a[0][2], a[0][3]);
#pragma unroll
  for (int i = 1; i < 5; ++i) {
    m0 = __builtin_fmaxf(__builtin_fmaxf(m0, a[i][0]), a[i][1]); m1 = __builtin_fmaxf(__builtin_fmaxf(m1, a[i][2]), a[i][3]);
    n0 = __builtin_fminf(__builtin_fminf(n0, a[i][0]), a[i][1]); n1 = __builtin_fminf(__builtin_fminf(n1, a[i][2]), a[i][3]);
  }
  mx = __builtin_fmaxf(m0, m1); mn = __builtin_fminf(n0, n1);
}
DI f32x2 lin2(const f32x4 (&a)[5], int i, float k, float c) { return __builtin_elementwise_fma(f32x2{a[i >> 1][(i & 1) * 2], a[i >> 1][(i & 1) * 2 + 1]}, f32x2{k, k}, f32x2{c, c}); }
DI void swap32(unsigned& a, unsigned& b) { const v2u r = __builtin_amdgcn_permlane32_swap(a, b, false, false); a = r.x; b = r.y; }
DI void swap16(unsigned& a, unsigned& b) { const v2u r = __builtin_amdgcn_permlane16_swap(a, b, false, false); a = r.x; b = r.y; }
DI void soft20(f32x2 (&e)[10], unsigned* w) {
  SB();
#pragma unroll
  for (int i = 0; i < 10; ++i) e[i] = f32x2{__builtin_amdgcn_exp2f(e[i].x), __builtin_amdgcn_exp2f(e[i].y)};
  SB();
  const f32x2 s = (((e[0] + e[1]) + (e[2] + e[3])) + ((e[4] + e[5]) + (e[6] + e[7]))) + (e[8] + e[9]);
  const float r = __builtin_amdgcn_rcpf(s.x + s.y);
#pragma unroll
  for (int i = 0; i < 10; ++i) { const f32x2 v = e[i] * f32x2{r, r}; w[i] = pk(v.x, v.y); }
}
DI void soft20s(float (&e)[20], unsigned* w) {
  SB();
#pragma unroll
  for (int i = 0; i < 20; ++i) e[i] = __builtin_amdgcn_exp2f(e[i]);
  SB();
  float s[4];
#pragma unroll
  for (int i = 0; i < 4; ++i) { s[i] = ((e[i] + e[4 + i]) + (e[8 + i] + e[12 + i])) + e[16 + i]; asm("" : "+v"(s[i])); }
  const float r = __builtin_amdgcn_rcpf((s[0] + s[1]) + (s[2] + s[3]));
#pragma unroll
  for (int i = 0; i < 10; ++i) { float x = e[2 * i] * r, y = e[2 * i + 1] * r; asm("" : "+v"(x)); asm("" : "+v"(y)); w[i] = pk(x, y); }
}
template <bool S> DI f32x4 add4(f32x4 a, f32x4 b) {
  if (!S) return a + b;
  float x = a[0] + b[0], y = a[1] + b[1], z = a[2] + b[2], w = a[3] + b[3];
  asm("" : "+v"(x)); asm("" : "+v"(y)); asm("" : "+v"(z)); asm("" : "+v"(w));
  return f32x4{x, y, z, w};
}
template <bool S> DI f32x4 mul4(f32x4 a, float b) {
  if (!S) return a * b;
  float x = a[0] * b, y = a[1] * b, z = a[2] * b, w = a[3] * b;
  asm("" : "+v"(x)); asm("" : "+v"(y)); asm("" : "+v"(z)); asm("" : "+v"(w));
  return f32x4{x, y, z, w};
}
template <int V> struct IC { static constexpr int v = V; };
template <int I, int N, class F> DI void sfor(F&& f) { if constexpr (I < N) { f(IC<I>{}); sfor<I + 1, N>(f); } }
struct Ctx {
  LAS char* L; rsrc_t rw; unsigned tag;
  int b, panel, cg, w, lane, rt, ct, head, tb, kb;
  const char* wp[4]; u64 wadv; const char* xb; unsigned xo0, xo1; int pitch, kst;
  int offN, offP, offX, offY; unsigned sa, sb, sc, fo, po;
  v4u sf[4]; unsigned sdst;
};
DI void piece(Ctx& c, int i, int dx, int dw) {
  if (i == 0) {
    const int d = c.kst == 9 ? -9 : 1;
    c.kst += d; c.xb += d * 128; c.wadv = (long long)c.pitch * (d * 64);
  }
  if (i < 2) {
    asm volatile("s_add_i32 m0, %2, %3\n\ts_nop 0\n\tglobal_load_lds_dwordx4 %0, %1" ::"v"(i ? c.xo1 : c.xo0), "s"(c.xb), "s"(dx), "n"(i * 1024) : "memory", "scc");
  } else {
    asm volatile("s_add_i32 m0, %2, %3\n\tv_lshl_add_u64 %0, %0, 0, %1\n\tglobal_load_lds_dwordx4 %0, off" : "+v"(c.wp[i - 2]) : "v"(c.wadv), "s"(dw), "n"((i - 2) * WR) : "memory", "scc");
  }
}
DI int img_col(int cg, int ch) {
  const int win = ((160 * (cg & 7)) & ~63) >> 1, j = cg - 8;
  const int b0 = cg < 8 ? D + win : win;
  const int b1 = cg < 8 ? 2 * D + win - 96 : 3 * D + (j < 7 ? 512 + 160 * j : 1600) - 96;
  const int b2 = cg < 8 ? 3 * D + 64 * cg - 192 : b1;
  const int cc = j >= 6 && ch > 27 ? 27 : ch;
  return (ch < 12 ? b0 : ch < 24 ? b1 : b2) + 8 * cc;
}

template <int NP, int NN, bool ISB, class TF>
DI void gemm1(const P& p, Ctx& c, int pbase, int nbase, f32x4 (&acc)[2][NP + NN], v4u (&wpre)[7][5], float (&mb)[2], float (&rs)[2], TF&& tail) {
  constexpr int NT = NP + NN;
  LAS char* L = c.L;
  const int lane = c.lane, w = c.w, g = lane >> 4, tok = lane & 15;
  const int offN = c.offN, offP = c.offP, offX = c.offX, offY = c.offY;
  const int xr = (w & 3) * 32 + tok;
  v2u wf[NT][2]; v4u xf[2][2];
  LAS char *aN, *aP, *aX0, *aX1;
  auto base = [&](int bo) { aN = L + offN + bo; aP = L + offP + bo; aX0 = L + offX + bo; aX1 = L + offY + bo; asm volatile("" : "+v"(aN), "+v"(aP), "+v"(aX0), "+v"(aX1)); };
  auto rd = [&](int s, int t, int h) { wf[t][h] = trd((t < NP ? aP + t * 8 : aN + (t - NP) * 32) + s * 512 + h * 16 * WR); };
  auto rdx = [&](int s, int a) { xf[s][a] = *(LAS v4u*)((s ? aX1 : aX0) + a * 2048); };
  auto mm = [&](int s, int t, int a, bool first = false) {
    const v4u f = {wf[t][0].x, wf[t][0].y, wf[t][1].x, wf[t][1].y};
    if (first) asm volatile("v_mfma_f32_16x16x32_bf16 %0, %1, %2, 0" : "=&v"(acc[a][t]) : "v"(f), "v"(xf[s][a]));
    else asm volatile("v_mfma_f32_16x16x32_bf16 %0, %1, %2, %0" : "+v"(acc[a][t]) : "v"(f), "v"(xf[s][a]));
  };
  constexpr int PR1 = 4, PR2 = 8, PR3 = NT + 4;
  bool last = false;
  auto prio = [&](int n) { if (last) { if (n == 0) __builtin_amdgcn_s_setprio(ISB ? 2 : NP ? TK : TQ); } else if (n == 0) __builtin_amdgcn_s_setprio(3); else if (n == PR1) __builtin_amdgcn_s_setprio(2); else if (n == PR2) __builtin_amdgcn_s_setprio(1); else if (n == PR3) __builtin_amdgcn_s_setprio(0); };
  auto k1b = [&](auto&& hook) {
#pragma unroll
    for (int t = 2; t < NT; ++t) {
      prio(t - 2);
      mm(1, t, 0);
      if (t == 2) { rdx(0, 0); rdx(0, 1); } else if (t == 3) { rd(0, 1, 0); rd(0, 1, 1); } else rd(0, t - 1, 0);
      mm(1, t, 1);
      if (t == 2) { rd(0, 0, 0); rd(0, 0, 1); } else if (t == 3) { rd(0, 2, 0); rd(0, 2, 1); } else rd(0, t - 1, 1);
      hook(t - 2);
    }
  };
  auto k0 = [&](auto&& hook, bool first = false) {
#pragma unroll
    for (int t = 0; t < NT; ++t) {
      prio(NT - 2 + t);
      mm(0, t, 0, first);
      if (t) rd(1, t - 1, 0); else if (!first) rd(0, NT - 1, 0);
      if (t == 1 || t == 2) rdx(1, t - 1);
      mm(0, t, 1, first);
      if (t) rd(1, t - 1, 1); else if (!first) rd(0, NT - 1, 1);
      hook(NT - 2 + t);
    }
    mm(1, 0, 0); rd(1, NT - 1, 0); mm(1, 0, 1); rd(1, NT - 1, 1);
  };
  auto close = [&]() { mm(1, 1, 0); mm(1, 1, 1); WAITL0(); BAR(); };
  int bo = 0, dx = 2 * BUF + w * 2048, dw = 2 * BUF + XB + w * 4 * WR;
#define RH 0, 2, 4, 6, 8, 10
  constexpr int rh[6] = {RH};
  auto ring = [&](int n) {
#pragma unroll
    for (int i = 0; i < 6; ++i) if (n == rh[i]) piece(c, i, dx, dw);
  };
  base(0);
  const unsigned sa = c.sa, sb = c.sb, sc = c.sc, fo = c.fo, po = c.po;
  v4u fl = {c.tag, c.tag, c.tag, c.tag}, em = {~0u, ~0u, ~0u, ~0u};
  if (ISB) asm volatile("" : "+v"(em), "+v"(fl));
  asm volatile("s_waitcnt vmcnt(%0)" ::"n"(NP1 + ISB) : "memory");
  WAITL0(); BAR(); SB();
  rdx(0, 0); rdx(0, 1);
#pragma unroll
  for (int t = 0; t < NT; ++t) { rd(0, t, 0); rd(0, t, 1); }
  k0([&](int n) {
    constexpr int R = 12 - NP1, E = R > 7 ? R - 7 : 0;
    const int h = n - (NT - 2), m0 = NP1 + (h < E ? 2 * h : E + h);
#pragma unroll
    for (int m = m0; m < m0 + (h < E ? 2 : 1); ++m) {
      if (m < 6) piece(c, m, BUF + w * 2048, BUF + XB + w * 4 * WR);
      else if (m < 12) piece(c, m - 6, dx, dw);
    }
  }, true);
  if (ISB) { st16(c.rw, sa, em); st16(c.rw, sa + 1024, em); st16(c.rw, sb, em); st16(c.rw, sc + 1024, em); }
  else if (w < 3) {
#pragma unroll
    for (int t = 0; t < 4; ++t) st16(c.rw, c.sdst + t * NKS * 1024, c.sf[t]);
  } else {
    LAS float* q = (LAS float*)(L + L_CP) + lane;
    st4(c.rw, sa, __builtin_bit_cast(unsigned, ((q[0] + q[64]) + (q[128] + q[192])) + q[256]));
    st4(c.rw, sb, *(LAS unsigned*)(L + L_ST + (lane & 7) * 4));
    WAITV(8);
  }
  WAITV(10);
#pragma unroll 1
  for (int s = 0; s < 7; ++s) {
    dx += bo - (bo == 0 ? 2 * BUF : bo - BUF); dw += bo - (bo == 0 ? 2 * BUF : bo - BUF);
    bo = bo == 2 * BUF ? 0 : bo + BUF;
    mm(1, 1, 0); SB(); base(bo); SB(); mm(1, 1, 1); WAITL0(); BAR();
    k1b([&](int n) {
      if (n == 0 && ISB) asm volatile("s_cmp_lg_u32 %4, %6\n\ts_cbranch_scc1 1f\n\tbuffer_store_dword %5, %2, %3, 0 offen sc1\n1:\ts_cmp_lg_u32 %4, 6\n\ts_cbranch_scc1 2f\n\tbuffer_load_dwordx4 %0, %1, %3, 0 offen sc1\n2:"
                               : "+v"(fl) : "v"(fo), "v"(po), "s"(c.rw), "s"(s + 8 * (w - 4)), "v"(c.tag), "n"(FLS) : "memory", "scc");
      ring(n);
    });
    k0(ring);
    WAITV(6);
  }
  const unsigned wo = WS_WT + (c.ct * 5 * NKS + (ISB ? 38 + 4 * w : c.kb)) * 1024 + lane * 16;
  constexpr int NW8 = ISB ? 10 : 2, NW9 = ISB ? 10 : 3;
  auto wl = [&](int n) { wpre[n / 5][n % 5] = ld16(c.rw, wo + ((n % 5) * NKS + n / 5) * 1024); };
  if (ISB) {
    asm volatile("" : "+v"(fl));
#pragma unroll 1
    for (unsigned i = 0; i < SPINS && __builtin_amdgcn_ballot_w64(fl.x != c.tag || fl.y != c.tag || fl.z != c.tag || fl.w != c.tag); ++i) { asm volatile("" ::: "memory"); fl = ld16(c.rw, fo); }
    if (c.b == 0 && w == 4) st4(c.rw, lane == 0 ? WS_EPOCH : OOB, c.tag);
  }
  base(2 * BUF);
  close();
  {
    const int tid = w * 64 + lane;
    const int col = img_col(c.cg, (tid >> 3) & 31) + (tid & 7);
    f32x4 cq; unsigned bb;
    k1b([&](int n) {
      if (n == 0) {
        if (ISB) asm volatile("buffer_load_dword %0, %1, %2, 0 offen sc1" : "=v"(bb) : "v"(WS_RS + (c.panel * 128 + (w & 3) * 32 + (lane & 31)) * 4), "s"(c.rw) : "memory");
        else asm volatile("buffer_load_dwordx4 %0, %2, %3, 0 offen sc1\n\tglobal_load_ushort %1, %4, %5" : "=&v"(cq), "=&v"(bb) : "v"(WS_CS + col * 16), "s"(c.rw), "v"((col >= 3 * D ? col - 3 * D : 0) * 2), "s"(p.b1) : "memory");
      } else if (n <= NW8) wl(n - 1);
    });
    k0([&](int n) { if (n <= NW8) wl(n - 1); });
    if (ISB) {
      asm volatile("s_waitcnt vmcnt(%1)" : "+v"(bb) : "n"(NW8) : "memory");
      *(LAS unsigned*)(L + L_RS + ((w & 3) * 32 + (lane & 31)) * 4) = bb;
    } else {
      asm volatile("s_waitcnt vmcnt(%2)" : "+v"(cq), "+v"(bb) : "n"(NW8) : "memory");
      *(LAS float*)(L + L_CS + tid * 4) = (cq[0] + cq[1]) + (cq[2] + cq[3]);
      *(LAS float*)(L + L_B1 + tid * 4) = lo(bb);
    }
    base(0);
    close();
  }
  {
    unsigned r0, r1;
    auto h9 = [&](int n) {
      if (n == 1) { r0 = *(LAS unsigned*)(L + L_RS + xr * 4); r1 = *(LAS unsigned*)(L + L_RS + xr * 4 + 64); }
      if (n == 6) {
        mb[0] = -lo(r0); rs[0] = __builtin_amdgcn_rcpf(hi(r0)); mb[1] = -lo(r1); rs[1] = __builtin_amdgcn_rcpf(hi(r1));
        asm volatile("" : "+v"(mb[0]), "+v"(mb[1]), "+v"(rs[0]), "+v"(rs[1]));
      }
      if (n < NW9) wl(NW8 + n);
      else if (!ISB && n < NW9 + 3) {
        const int ch = (n - NW9) * 64 + lane, r = (ch * 205) >> 11;
        const char* q = ch < 160 ? (const char*)p.x + (size_t)((c.rt ^ 1) * 64 + ((w + RTA(c.ct)) & 3) * 16) * (D * 2) + (r * (D * 2 - 160) + ch * 16) : (const char*)p.b2 + (ch < 170 ? ch - 160 : 0) * 16;
        glds(q + c.ct * 160, L_RES + w * 3072 + (n - NW9) * 1024);
      }
    };
    last = LT == 1;
    k1b(h9);
    if (LT == 2) { last = true; prio(0); }
    k0(h9);
    if (LT == 3) { last = true; prio(0); }
#pragma unroll
    for (int t = 1; t < NT; ++t) { mm(1, t, 0); mm(1, t, 1); tail(t - 1); }
    __builtin_amdgcn_s_setprio(ISB ? PB0 : NP ? PAK : PAQ);
    asm volatile("s_nop 15" ::: "memory");
    SB();
  }
}
DI void hconst(Ctx& c, int pos, f32x4 (&k)[4]) {
  LAS char* t = c.L + pos * 2 + (c.lane >> 4) * 16;
  k[0] = *(LAS f32x4*)(t + L_CS); k[1] = *(LAS f32x4*)(t + L_CS + 64); k[2] = *(LAS f32x4*)(t + L_B1); k[3] = *(LAS f32x4*)(t + L_B1 + 64);
}
DI void hidden(Ctx& c, const f32x4& a00, const f32x4& a01, const f32x4& a10, const f32x4& a11, const f32x4 (&k)[4], unsigned dst, const float (&mb)[2], const float (&rs)[2]) {
#pragma unroll
  for (int a = 0; a < 2; ++a) {
    const float n = mb[a] * rs[a];
    f32x4 u = (a ? a10 : a00) * rs[a] + (k[0] * n + k[2]), v = (a ? a11 : a01) * rs[a] + (k[1] * n + k[3]);
#pragma unroll
    for (int i = 0; i < 4; ++i) { u[i] *= __builtin_fmaxf(u[i], 0.f); v[i] *= __builtin_fmaxf(v[i], 0.f); }
    st16(c.rw, dst + a * NKS * 1024, pk8(u, v));
  }
}

template <bool ISB>
DI void run(const P& p, const int w, unsigned (&cw)[4]) {
  extern __shared__ __attribute__((aligned(16))) char smem[];
  LAS char* L = (LAS char*)smem;
  Ctx c;
  const int b = blockIdx.x, xcd = b & 7, r = b >> 3;
  if (K1MAP == 0) { c.panel = xcd * 2 + (r >> 4); c.cg = r & 15; }
  else if (K1MAP == 1) { c.panel = (xcd >> 1) * 4 + (r >> 3); c.cg = (r & 3) + (xcd & 1) * 4 + ((r >> 2) & 1) * 8; }
  else { c.panel = (xcd >> 2) * 8 + (r >> 2); c.cg = (r & 1) + (xcd & 3) * 2 + ((r >> 1) & 1) * 8; }
  if (K2MAP == 0) { c.rt = xcd * 4 + (r >> 3); c.ct = r & 7; }
  else if (K2MAP == 1) { c.rt = (xcd >> 1) * 8 + (r >> 2); c.ct = (xcd & 1) * 4 + (r & 3); }
  else { c.rt = (xcd >> 1) * 8 + (r >> 3) * 2 + ((xcd ^ XFLIP) & 1); c.ct = r & 7; }
  const int tid = threadIdx.x, lane = tid & 63;
  const int g = lane >> 4, tok = lane & 15, panel = c.panel, cg = c.cg, pid = panel * 16 + cg;
  const bool KV = cg < 8;
  c.L = L; c.b = b; c.w = w; c.lane = lane;
  c.head = 4 * xcd + (r & 3); c.tb = r >> 2;
  c.kb = ((c.rt ^ (HPAR ? c.ct : 0)) & 1 ? 26 + (c.rt & 1) * 2 : 0) + 7 * w - (w == 3 ? c.rt & 1 : 0);
  c.rw = __builtin_amdgcn_make_buffer_rsrc(p.ws, 0, WS_END, 0x00020000);
  unsigned ep; v4u xa, xc;
  {
    const char* row = (const char*)p.x + (size_t)(panel * 128 + cg * 8 + w) * (D * 2);
    asm volatile("s_load_dword %0, %1, 0" : "=s"(ep) : "s"(p.ws) : "memory");
    asm volatile("global_load_dwordx4 %0, %1, %2" : "=v"(xa) : "v"(lane * 16), "s"(row) : "memory");
    asm volatile("global_load_dwordx4 %0, %1, %2 offset:1024" : "=v"(xc) : "v"(tok * 16), "s"(row) : "memory");
  }
  const int pks0 = 3 * (b / 10) + w, pks = pks0 < NKS ? pks0 : NKS - 1, pcb = b % 10;
  {
    const char* src; unsigned o0, st;
    if (!ISB && w < 3) {
      if (pks < 54) { src = (const char*)p.w2 + (size_t)(32 * (pks < 8 || pks >= 36 ? pks : pks < 28 ? pks + 8 : pks - 20)) * (D * 2); o0 = (lane >> 3) * (D * 2); st = 8 * D * 2; }
      else if (pks < 70) { src = (const char*)p.wo + (size_t)(40 * (pks - 54)) * (D * 2); o0 = ((lane >> 5) * 8 + ((lane >> 3) & 3)) * (D * 2); st = 20 * D * 2; }
      else { src = (const char*)p.wo + (size_t)(160 * (pks - 70) + 16) * (D * 2); o0 = ((lane >> 5) * 40 + ((lane >> 3) & 3)) * (D * 2); st = 80 * D * 2; }
      src += pcb * 128;
    } else {
      const int jb = b < 228 ? b : 0, lc = jb % 57, k0 = (jb / 57) * 160 + (w - 3) * 32;
      if (lc < 30) { src = (const char*)p.wqkv + (size_t)k0 * (3 * D * 2) + lc * 128; st = 8 * 3 * D * 2; }
      else { src = (const char*)p.w1 + (size_t)k0 * (F * 2) + (lc - 30) * 128; st = 8 * F * 2; }
      o0 = (lane >> 3) * (st >> 3);
    }
    o0 += (lane & 7) * 16;
    const unsigned st2 = (!ISB && w < 3 && pks >= 54) ? (pks < 70 ? 4 * D * 2 : 20 * D * 2) : 2 * st;
    const unsigned dst = 2 * BUF + w * 4096;
    gldso(src, o0, dst); gldso(src, o0 + st, dst + 1024); gldso(src, o0 + st2, dst + 2048); gldso(src, o0 + st2 + st, dst + 3072);
  }
  {
    const int col = img_col(cg, lane & 31), k = (lane >> 5) * 32 + (w & 3) * 8 + (w >> 2) * 4;
    const bool f1 = col >= 3 * D;
    const unsigned pitch = f1 ? F * 2 : 3 * D * 2;
    const char* q = (f1 ? (const char*)p.w1 - 3 * D * 2 : (const char*)p.wqkv) + (k * pitch + col * 2);
    constexpr int ro[4] = {ROT};
    const int ri = (panel + cg) & 3;
    c.kst = (ri == 0 ? ro[0] : ri == 1 ? ro[1] : ri == 2 ? ro[2] : ro[3]) - 1; c.pitch = pitch;
#pragma unroll
    for (int i = 0; i < 4; ++i) c.wp[i] = q + (i + c.kst * 64) * (int)pitch;
    c.xb = (const char*)p.x + (size_t)(panel * 128 + w * 16) * (D * 2) + c.kst * 128;
    c.xo0 = (lane >> 3) * (D * 2) + 16 * ((lane & 7) ^ (lane >> 4)); c.xo1 = (8 + (lane >> 3)) * (D * 2) + 16 * ((lane & 7) ^ (4 | (lane >> 4)));
  }
#pragma unroll
  for (int i = 0; i < 6; ++i) piece(c, i, w * 2048, XB + w * 4 * WR);
  if (ISB && w == 7) *(LAS v4u*)(L + L_ZERO + lane * 16) = v4u{0, 0, 0, 0};
  if (!ISB && tid < 24) *(LAS unsigned*)(L + L_CNT + tid * 4) = 0;
  {
  const int sn = ISB ? 4 * w - 16 - (w == 7 ? 2 : 0) : 0;
  unsigned sa = (sn < 10 ? WS_AFR + ((c.b >> 1) * NKS + 54 + 10 * (c.b & 1) + sn) * 1024 : WS_KVP + c.b * (2 * KVB) + (sn - 10) * 1024) + lane * 16, sb = sn == 8 ? OOB : sa + 2048, sc = sn == 10 && lane >= 32 ? OOB : sb;
  if (!ISB && w == 3) { sa = c.b < 228 ? WS_CS + ((c.b % 57) * 64 + lane) * 16 + (c.b / 57) * 4 : OOB; sb = lane < 8 ? WS_RS + (c.panel * 128 + c.cg * 8 + lane) * 4 : OOB; }
  unsigned fo = FL_PT + lane * 16, po = lane == 0 ? FL_PT + c.b * 4 : OOB;
  const int dd = (cg & 1) * 32, xr = (w & 3) * 32 + tok;
  c.offN = XB + (lane >> 2) * WR + 8 * (lane & 3) + (ISB ? (KV ? 448 : 192) : (KV ? 384 : 256)); c.offP = XB + (lane >> 2) * WR + 40 * (lane & 3) + (ISB ? dd : 192 + dd);
  c.offX = xr * 128 + 16 * (g ^ ((xr >> 1) & 7)); c.offY = c.offX ^ 64;
  c.sdst = pks0 < NKS && b < NWT ? WS_WT + (4 * pcb * NKS + pks) * 1024 + lane * 16 : OOB;
  asm volatile("" : "+v"(sa), "+v"(sb), "+v"(c.offN), "+v"(c.offP), "+v"(c.offX), "+v"(c.offY));
  if (ISB) asm volatile("" : "+v"(sc), "+v"(fo), "+v"(po)); else asm volatile("" : "+v"(c.sdst));
  c.sa = sa; c.sb = sb; c.sc = sc; c.fo = fo; c.po = po;
  }
  asm volatile("s_waitcnt vmcnt(6) lgkmcnt(0)" : "+s"(ep), "+v"(xa), "+v"(xc), "+s"(cw[0]), "+s"(cw[1]), "+s"(cw[2]), "+s"(cw[3])::"memory");
#pragma unroll
  for (int t = 0; t < 4; ++t) c.sf[t] = trd2(L + 2 * BUF + w * 4096 + (lane >> 2) * 128 + (lane & 3) * 8 + t * 32, 16 * 128);
  c.tag = ep + 1u;
#pragma unroll
  for (int i = 0; i < NP0; ++i) piece(c, i, BUF + w * 2048, BUF + XB + w * 4 * WR);
  {
    if (lane >= 16) xc = v4u{0, 0, 0, 0};
    float s0 = 0.f, s1 = 0.f, s2 = 0.f, s3 = 0.f;
#pragma unroll
    for (int i = 0; i < 4; ++i) { s0 = dot2(xa[i], 0x3f803f80u, s0); s1 = dot2(xa[i], xa[i], s1); s2 = dot2(xc[i], 0x3f803f80u, s2); s3 = dot2(xc[i], xc[i], s3); }
    s0 = wave_sum(s0 + s2) * (1.f / D); s1 = wave_sum(s1 + s3) * (1.f / D);
    *(LAS unsigned*)(L + L_ST + w * 4) = pk(s0, __builtin_sqrtf(rb(rb(__builtin_fmaxf(s1 - s0 * s0, 0.f)) + 1e-5f)));
  }
  if (ISB || w == 3) {
    const v4u ones = {0x3f803f80u, 0x3f803f80u, 0x3f803f80u, 0x3f803f80u};
    f32x4 cs[4];
#pragma unroll
    for (int t = 0; t < 4; ++t) cs[t] = mfma32(c.sf[t], ones, f32x4{0, 0, 0, 0});
    const f32x4 v = tok == 0 ? cs[0] : tok == 1 ? cs[1] : tok == 2 ? cs[2] : cs[3];
    if (tok < 4) *(LAS f32x4*)(L + L_CP + (w - 3) * 256 + tok * 64 + g * 16) = v;
  }
  SB();
#pragma unroll
  for (int i = NP0; i < NP1; ++i) piece(c, i, BUF + w * 2048, BUF + XB + w * 4 * WR);
  if (ISB && lane < 50) gldso((const char*)p.wphi + (cg & 7) * 3200 + (w - 4) * 800, lane * 16, L_WPHI + (w - 4) * 800);

  v4u wpre[7][5];
  float mb[2], rs[2];
  const int dd = (cg & 1) * 32, rt = c.rt, ct = c.ct, aw = w & 3;
  const unsigned afr = WS_AFR + rt * 4 * NKS * 1024 + lane * 16, wt = WS_WT + ct * 5 * NKS * 1024 + lane * 16;
  const unsigned fh = sel(lane < 8, MB_H + (panel * 16 + (cg & 4) * 2 + lane) * 256 + ((cg & 3) + (cg >> 3) * 4) * 4, OOB);
  const unsigned hd0 = WS_AFR + ((panel * 8 + aw * 2) * NKS + ((cg >> 2) == 1 ? 20 : (cg >> 2) == 2 ? -8 : 0)) * 1024 + lane * 16;
  auto pack = [&](f32x4 (&v)[7], v4u (&bq)[3]) {
    bq[0] = pk8(v[0], v[1]); bq[1] = pk8(v[2], v[3]);
    const v2u e = pk4(v[4]); bq[2] = v4u{e.x, e.y, 0x00003f80u, 0u};
  };
  if (!ISB) {
    if (!KV) {
      f32x4 acc[2][8], hk[4][4];
      unsigned hd, hl;
      gemm1<0, 8, false>(p, c, 0, 256, acc, wpre, mb, rs, [&](int n) {
        if (n < 4) hconst(c, 256 + 64 * n, hk[n]);
        if (n == 4) { hd = hd0 + (cg < 15 ? 5 * cg - 23 : 51) * 1024; hl = cg < 14 ? hd : OOB; asm volatile("" : "+v"(hd), "+v"(hl)); }
      });
#pragma unroll
      for (int u = 0; u < 4; ++u) hidden(c, acc[0][2 * u], acc[0][2 * u + 1], acc[1][2 * u], acc[1][2 * u + 1], hk[u], (u < 3 ? hd : hl) + u * 1024, mb, rs);
    } else {
      f32x4 acc[2][7], hk[4], cs[5];
      unsigned hd; LAS char* d;
      gemm1<5, 2, false>(p, c, 192 + dd, 384, acc, wpre, mb, rs, [&](int n) {
        if (n == 0) hconst(c, 384, hk);
        if (n == 1) {
#pragma unroll
          for (int t = 0; t < 5; ++t) cs[t] = *(LAS f32x4*)(L + L_CS + (192 + dd) * 2 + g * 80 + t * 16);
        }
        if (n == 2) { hd = hd0 + 2 * cg * 1024; d = L + L_V + (w * 32 + tok) * PV + g * 48; asm volatile("" : "+v"(hd), "+v"(d)); }
      });
      hidden(c, acc[0][5], acc[0][6], acc[1][5], acc[1][6], hk, hd, mb, rs);
      SB();
      if (PAV != PAK) __builtin_amdgcn_s_setprio(PAV);
#pragma unroll
      for (int a = 0; a < 2; ++a) {
        v4u bq[3];
        const float n = mb[a] * rs[a];
#pragma unroll
        for (int t = 0; t < 5; ++t) acc[a][t] = acc[a][t] * rs[a] + cs[t] * n;
        pack(acc[a], bq);
        *(LAS v4u*)(d + a * 16 * PV) = bq[0]; *(LAS v4u*)(d + a * 16 * PV + 16) = bq[1]; *(LAS v4u*)(d + a * 16 * PV + 32) = bq[2];
      }
      arrive(C_V);
    }
    const bool full = w < 2 || !(rt & 1);
    const unsigned bp = wt + c.kb * 1024, bpl = full ? bp : OOB;
    unsigned ap = WS_AFR + ((rt ^ 1) * 4 * NKS + c.kb) * 1024 + lane * 16, apl = full ? ap : OOB;
    auto pld = [&](unsigned off, unsigned so) { SB(); asm volatile("s_waitcnt vmcnt(%0)" ::"n"(INF - 1)); const v4u v = ld16s(c.rw, off, so); SB(); return v; };
    SB();
    __builtin_amdgcn_s_setprio(PA0);
#pragma unroll
    for (int n = 5; n < 8; ++n) wpre[n / 5][n % 5] = ld16s(c.rw, bp, ((n % 5) * NKS + n / 5) * 1024);
    SB();
    WAITV(3);
    arrive_flag(C_H, 7, c.rw, fh, c.tag);
    const unsigned mb0 = (rt * 8 + ct) * 256;
    f32x4 acc[2][5], t[5], u[5];
    {
      v8u fv;
#pragma unroll
      for (int n = 8; n < 35; ++n) {
        if (n == 34 - FLEAD) await(L, C_H, 8);
        if (n == 35 - FLEAD) asm volatile("s_load_dwordx8 %0, %1, %2 glc" : "=s"(fv) : "s"(p.ws), "s"(MB_H + mb0) : "memory");
        wpre[n / 5][n % 5] = pld(n < 30 ? bp : bpl, ((n % 5) * NKS + n / 5) * 1024);
      }
      asm volatile("s_waitcnt lgkmcnt(0)" : "+s"(fv)::"memory");
      if (differs<8>(fv, c.tag)) wait_flags<8>(p.ws, c.rw, MB_H + mb0, 0, c.tag);
    }
    LAS char* stg = L + w * AW + lane * 16;
    LAS char* q0 = L + ((w + 3) & 3) * AW + lane * 16;
    LAS char* q1 = L + ((w + 2) & 3) * AW + 5120 + lane * 16;
    LAS char* q2 = L + ((w + 1) & 3) * AW + 10240 + lane * 16;
    LAS char* qs = L + L_XB + w * 15360 + 10240 + lane * 16;
    v4u ra[RA];
    unsigned so[4], cn, cd;
#pragma unroll
    for (int i = 0; i < 4; ++i) so[i] = ((w + RTA(ct) + 1 + i) & 3) * NKS * 1024;
    const int dr = (rt & 1 ? 4 : -4) * NKS * 1024;
    auto ald = [&](int m) { if (m < INF) return pld(m % 7 == 6 ? apl : ap, so[m / 7 % 4] + m % 7 * 1024); SB(); const v4u v = ld16s(c.rw, m % 7 == 6 ? apl : ap, so[m / 7 % 4] + m % 7 * 1024); SB(); return v; };
#pragma unroll
    for (int m = 0; m < RA; ++m) ra[m] = ald(m);
    sfor<0, 8>([&](auto N) {
      constexpr int n = N.v, a = n & 1, i = n & 3, th = n < 4 ? 4 : 8;
#pragma unroll
      for (int k = 0; k < 7; ++k) {
#pragma unroll
        for (int j = 0; j < 5; ++j) {
          acc[a][j] = mfma32(wpre[k][j], ra[(n * 7 + k) % RA], k || i == 3 ? acc[a][j] : f32x4{0, 0, 0, 0});
          SB();
          const int m = k * 5 + j, f = n * 7 + k - 1 + RA;
          if (j == LDJ && n + k && f < 56) {
            if (f == 28) { ap += dr; apl += dr; }
            ra[f % RA] = ald(f);
          }
          if (RPOS && n * 35 + m == RPOS) arrive(C_R);
          auto at = [&](int m0, int st) { const int d = m - m0 - (m + 4 - LDJ) / 5 + (m0 + 4 - LDJ) / 5; return j != LDJ && d >= 0 && d % st == 0 && d / st < 5 ? d / st : -1; };
          if (i) { const int x = at(WS0, WST); if (x >= 0) *(LAS f32x4*)(stg + ((i - 1) * 5 + x) * 1024) = acc[a ^ 1][x]; if (at(WS0 + 1, WST) == 4) arrive(C_A + 4 * (i - 1)); }
          if (i == 2) {
            if (m == 17) cn = peekv(L, C_A);
            if (m == 22) recheck(cn, C_A, th);
            if (at(23, 1) >= 0) acc[a ^ 1][at(23, 1)] = *(LAS f32x4*)(q0 + at(23, 1) * 1024);
          }
          if (i == 3) {
            if (m == TP) cn = peekv(L, C_A + 4);
            if (m == TP + 5) recheck(cn, C_A + 4, th);
            if (at(TP + 6, 1) >= 0) t[at(TP + 6, 1)] = *(LAS f32x4*)(q1 + at(TP + 6, 1) * 1024);
            if (m >= TA && m < TA + 5) acc[1][m - TA] = add4<SC5>(acc[1][m - TA], t[m - TA]);
          }
          if (n == 3 && m == 28) cd = peekv(L, C_D);
          if (n == 3 && m == 29) cn = peekv(L, C_A + 8);
          if (n == 3 && m == 33) recheck(cd, C_D, 4);
          if (n == 3 && m == 34) recheck(cn, C_A + 8, 4);
          if (n == 4) {
            if (at(0, 1) >= 0) u[at(0, 1)] = *(LAS f32x4*)(q2 + at(0, 1) * 1024);
            if (at(7, 1) >= 0) *(LAS f32x4*)(qs + at(7, 1) * 1024) = add4<SC5>(acc[1][at(7, 1)], u[at(7, 1)]);
            if (m == 13) arrive(C_S + 4 * w);
          }
          if (n == 5 && m == 27) cn = peekv(L, C_S + 4 * (w ^ 2));
          if (n == 5 && m == 33) recheckv(cn, C_S + 4 * (w ^ 2), 1);
          if (n == 7) {
            if (m == 22) cn = peekv(L, C_A + 8);
            if (m == 28) recheck(cn, C_A + 8, 8);
            if (m >= 29 && m < 34) u[m - 29] = *(LAS f32x4*)(q2 + (m - 29) * 1024);
          }
          SB();
        }
      }
      SB();
    });
#pragma unroll
    for (int j = 0; j < 5; ++j) *(LAS f32x4*)(q0 + j * 1024) = acc[1][j] + u[j];
    asm volatile("hk_end0:");
    arrive(C_X + 4 * w);
  } else {
    const int head = c.head, tb = c.tb;
    const unsigned qf = MB_Q + (head * 8 + tb) * 128 + (aw >> 1) * 4, kf = MB_K + (head * 8 + tb) * 128, qo = WS_PHQ + (head * 32 + tb * 4 + aw) * 3072;
    unsigned fq = c.tag - 1, fq2 = c.tag - 1, kl = 1;
    {
      f32x4 acc[2][7], pq[2][5], hk[4], cs[5];
      unsigned hd; LAS char* wp;
      gemm1<5, 2, true>(p, c, dd, KV ? 448 : 192, acc, wpre, mb, rs, [&](int n) {
        if (n == 0) hconst(c, KV ? 448 : 192, hk);
        if (n == 2) {
          hd = hd0 + (KV ? 2 * cg + 1 : (cg < 15 ? 5 * cg - 24 : 50)) * 1024;
          wp = L + sel((lane & 3) == g, L_WPHI + g * 800 + ((lane & 15) >> 2) * 40, L_ZERO);
          asm volatile("" : "+v"(hd), "+v"(wp));
        }
      });
      {
        v4u a0[5], a1[5], bq[2][3]; v2u a2[5];
#pragma unroll
        for (int t = 0; t < 5; ++t) cs[t] = *(LAS f32x4*)(L + L_CS + dd * 2 + g * 80 + t * 16);
        if (HQ == 0 || (HQ == 1 && KV)) hidden(c, acc[0][5], acc[0][6], acc[1][5], acc[1][6], hk, hd, mb, rs);
        SB();
#pragma unroll
        for (int t = 0; t < 5; ++t) { a0[t] = trd2(wp + t * 8, 160); a1[t] = trd2(wp + 320 + t * 8, 160); a2[t] = trd(wp + 640 + t * 8); }
#pragma unroll
        for (int a = 0; a < 2; ++a) {
#pragma unroll
          for (int t = 0; t < 5; ++t) acc[a][t] += cs[t] * mb[a];
          pack(acc[a], bq[a]);
        }
        SB();
#pragma unroll
        for (int a = 0; a < 2; ++a) {
#pragma unroll
          for (int t = 0; t < 5; ++t) pq[a][t] = mfma32(a0[t], bq[a][0], f32x4{0, 0, 0, 0});
#pragma unroll
          for (int t = 0; t < 5; ++t) pq[a][t] = mfma32(a1[t], bq[a][1], pq[a][t]);
#pragma unroll
          for (int t = 0; t < 5; ++t) pq[a][t] = mfma16(a2[t], v2u{bq[a][2].x, bq[a][2].y}, pq[a][t]);
        }
      }
      SB();
      if (HQ == 2 && KV) { hidden(c, acc[0][5], acc[0][6], acc[1][5], acc[1][6], hk, hd, mb, rs); SB(); }
      if (KV ? KH == 0 : QH == 0 && HQ == 0) {
        WAITV(0);
        arrive_flag(C_H, 7, c.rw, fh, c.tag);
      }
      constexpr float L2E = 1.4426950408889634f;
      if (KV) {
#pragma unroll
        for (int a = 0; a < 2; ++a) {
          unsigned wv[20]; float mx, mn; f32x2 e[10];
          if (a && Q1 == 1) asm volatile("s_load_dword %0, %1, %2 glc" : "=s"(fq) : "s"(p.ws), "s"(qf) : "memory");
          range20(pq[a], mx, mn);
          const float k = L2E * rs[a];
#pragma unroll
          for (int i = 0; i < 10; ++i) e[i] = lin2(pq[a], i, k, mx * -k);
          soft20(e, wv);
          if (a && Q1 == 2) asm volatile("s_load_dword %0, %1, %2 glc" : "=s"(fq) : "s"(p.ws), "s"(qf) : "memory");
#pragma unroll
          for (int i = 0; i < 10; ++i) e[i] = lin2(pq[a], i, -k, mn * k);
          soft20(e, wv + 10);
          LAS char* d = L + L_PHK + ((aw * 2 + a) * 16 + tok) * PK + g * 80;
          *(LAS v4u*)(d) = v4u{wv[0], wv[1], wv[4], wv[5]};
          *(LAS v4u*)(d + 16) = v4u{wv[8], wv[9], wv[12], wv[13]};
          *(LAS v4u*)(d + 32) = v4u{wv[2], wv[3], wv[6], wv[7]};
          *(LAS v4u*)(d + 48) = v4u{wv[10], wv[11], wv[14], wv[15]};
          *(LAS v4u*)(d + 64) = v4u{wv[16], wv[17], wv[18], wv[19]};
          SB();
          if (a + 1 == KH) { WAITV(0); arrive_flag(C_H, 7, c.rw, fh, c.tag); }
        }
        arrive(C_V);
        asm volatile("s_waitcnt lgkmcnt(0)" : "+s"(fq)::"memory");
      } else {
#pragma unroll
        for (int a = 0; a < 2; ++a) {
          unsigned wv[12]; float mx, mn;
          range20(pq[a], mx, mn);
          const float iv = __builtin_amdgcn_rcpf(__builtin_fmaxf(mx - mn, 1e-30f));
#pragma unroll
          for (int i = 0; i < 10; ++i) { const f32x2 v = lin2(pq[a], i, iv, mn * -iv); wv[i] = __builtin_bit_cast(unsigned, __builtin_amdgcn_cvt_pknorm_u16(v.x, v.y)); }
          wv[10] = wv[11] = __builtin_bit_cast(unsigned, (mx - mn) * rs[a] * (L2E / 65535.f));
          const unsigned d = WS_PHQ + (((cg - 8) * 4 + g) * 32 + panel * 2 + (aw >> 1)) * 3072 + (((aw & 1) * 2 + a) * 16 + tok) * 16;
#pragma unroll
          for (int i = 0; i < 3; ++i) st16(c.rw, d + i * 1024, v4u{wv[4 * i], wv[4 * i + 1], wv[4 * i + 2], wv[4 * i + 3]});
          SB();
          if (a + 1 == QH) { asm volatile("s_waitcnt vmcnt(%0)" ::"n"(3 * QH) : "memory"); arrive_flag(C_H, 7, c.rw, fh, c.tag); }
        }
        if (HQ) { hidden(c, acc[0][5], acc[0][6], acc[1][5], acc[1][6], hk, hd, mb, rs); SB(); }
      }
    }
    v4u pu[3];
    if (KV) {
      asm volatile("s_cmp_lg_u32 %3, %4\n\ts_cbranch_scc1 1f\n\t" Q_LOADS "\n1:" : "=&v"(pu[0]), "=&v"(pu[1]), "=&v"(pu[2]) : "s"(fq), "s"(c.tag), [qv] "v"(lane * 16), [rw] "s"(c.rw), [qo] "s"(qo) : "memory", "scc");
      recheck(peekv(L, C_V), C_V, 8);
      f32x4 kv[2][3]; v16u fk;
      LAS char* A = L + L_PHK + (lane >> 2) * PK + aw * 80 + (lane & 3) * 8;
      LAS char* Bv = L + L_V + (lane >> 2) * PV + aw * 48 + (lane & 3) * 16;
#pragma unroll
      for (int s = 0; s < 4; ++s) {
        if (s == Q2) { asm volatile("s_load_dword %0, %2, %3 glc\n\ts_load_dwordx16 %1, %2, %4 glc" : "=&s"(fq2), "=&s"(fk) : "s"(p.ws), "s"(qf), "s"(kf) : "memory"); }
        const v4u b0 = trd2(Bv + s * 32 * PV, 16 * PV), b1 = trd2(Bv + 8 + s * 32 * PV, 16 * PV);
#pragma unroll
        for (int T = 0; T < 3; ++T) {
          const v4u f = trd2(A + s * 32 * PK + T * 32, 16 * PK);
          kv[0][T] = mfma32(f, b0, s ? kv[0][T] : f32x4{0, 0, 0, 0}); kv[1][T] = mfma32(f, b1, s ? kv[1][T] : f32x4{0, 0, 0, 0});
        }
      }
      if (Q2 == 4) { SB(); asm volatile("s_load_dword %0, %2, %3 glc\n\ts_load_dwordx16 %1, %2, %4 glc" : "=&s"(fq2), "=&s"(fk) : "s"(p.ws), "s"(qf), "s"(kf) : "memory"); }
      const unsigned d = WS_KVP + ((cg * 4 + aw) * 16 + panel) * KVB;
      const unsigned d0 = tok < 12 ? d + (g * 12 + tok) * 16 : OOB, d1 = tok < 9 ? d + (48 + g * 9 + tok) * 16 : OOB;
      st16(c.rw, d0, pk8(kv[0][0], kv[0][1]));
      st16(c.rw, d1, pk8(kv[1][0], kv[1][1]));
      st16(c.rw, (lane < 32 ? d0 : OOB) + 84 * 16, pk8(kv[0][2], kv[1][2]));
      SB();
      st4(c.rw, sel(lane < 8, MB_K + ((cg * 4 + aw) * 8 + lane) * 128 + panel * 4, OOB), c.tag);
      asm volatile("s_waitcnt vmcnt(4)" : "+v"(pu[0]), "+v"(pu[1]), "+v"(pu[2])::"memory");
      if (Q2 < 5) {
        asm volatile("s_waitcnt lgkmcnt(0)" : "+s"(fq2), "+s"(fk)::"memory");
        kl = differs<16>(fk, c.tag);
      }
    } else {
      asm volatile("" : "=v"(pu[0]), "=v"(pu[1]), "=v"(pu[2]));
      if (HQ && HW) WAITV(2); else WAITV(0);
      arrive_flag<HQ && HW>(C_Q, 3, c.rw, sel(lane < 4, MB_Q + (((cg - 8) * 4 + lane) * 8 + (panel >> 1)) * 128 + (panel & 1) * 4, OOB), c.tag);
      if (HQ) { arrive_flag(C_H, 7, c.rw, fh, c.tag); }
    }
    arrive(C_D);
    v8u fo8;
    {
      const int lane = lane_id(), g = lane >> 4, tok = lane & 15;
      v16u fv; unsigned t0, t1, it;
      asm volatile("s_cmp_eq_u32 %2, %3\n\ts_cbranch_scc1 2f\n\ts_mov_b32 %1, 0x100000\n1:\ts_cmp_eq_u32 %0, %3\n\ts_cbranch_scc1 2f\n\ts_load_dword %0, %4, %5 glc\n\ts_sub_u32 %1, %1, 1\n\ts_waitcnt lgkmcnt(0)\n\ts_cbranch_scc0 1b\n2:"
                   : "+s"(fq2), "=&s"(it) : "s"(fq), "s"(c.tag), "s"(p.ws), "s"(qf) : "memory", "scc");
      asm volatile("s_cmp_eq_u32 %3, %4\n\ts_cbranch_scc1 1f\n\t" Q_LOADS "\n1:" : "+v"(pu[0]), "+v"(pu[1]), "+v"(pu[2]) : "s"(fq), "s"(c.tag), [qv] "v"(lane * 16), [rw] "s"(c.rw), [qo] "s"(qo) : "memory", "scc");
      asm volatile("s_cmp_eq_u32 %3, 0\n\ts_cbranch_scc1 1f\n\ts_load_dwordx16 %0, %1, %2 glc\n1:" : "=&s"(fv) : "s"(p.ws), "s"(kf), "s"(fq == c.tag ? 0u : kl) : "memory", "scc");
      const int ci = lane & 31, sj = (tok & 3) + 4 * (tok >> 3);
      unsigned ko = sel(ci < 27, WS_KVP + (head * 16 + (lane >> 5) * 8) * KVB + (aw * 27 + ci) * 16, OOB);
      int kw = L_KV + (ci < 27 ? aw * 27 + ci : 108) * 16 + (lane >> 5) * 8;
      const unsigned sv = (g & 1) == ((tok >> 2) & 1) ? 0x3f80u << (sj & 1) * 16 : 0u;
      v4u sl = {sj >> 1 == 0 ? sv : 0u, sj >> 1 == 1 ? sv : 0u, sj >> 1 == 2 ? sv : 0u, sj >> 1 == 3 ? sv : 0u};
      const int c0 = tok < 12 ? tok : 0, c1 = tok < 8 ? tok : 8;
      int r0 = L_KV + (g * 12 + c0) * 16, r1 = L_KV + (48 + g * 9 + c1) * 16, r2 = L_KV + (84 + c0) * 16, r3 = L_KV + (84 + c1) * 16 + 8;
      const unsigned ob = WS_AFR + (tb * 16 + aw * 4) * NKS * 1024;
      unsigned oa = ob + ((lane >> 5) * NKS + 54 + (head >> 1)) * 1024 + (head & 1) * 512 + ci * 16;
      unsigned oc = ob + (((g & 1) * 2 + (g >> 1)) * NKS + 70 + (head >> 3)) * 1024 + ((head >> 1) & 3) * 256 + tok * 16 + (head & 1) * 8;
      unsigned of = sel(lane < 8, MB_O + ((tb * 4 + aw) * 8 + lane) * 256 + head * 4, OOB);
      asm volatile("" : "+v"(ko), "+v"(kw), "+v"(sl), "+v"(r0), "+v"(r1), "+v"(r2), "+v"(r3), "+v"(oa), "+v"(oc), "+v"(of));
      v16u ka, kb;
      asm volatile("" : "={v[224:239]}"(ka), "={v[240:255]}"(kb));
      asm volatile("s_waitcnt lgkmcnt(0)" : "+s"(fv)::"memory");
      const bool late = kl && (fq == c.tag || differs<16>(fv, c.tag));
      if (!late) { K_ISSUE(); asm volatile("s_cmp_eq_u32 %0, %1\n\ts_cbranch_scc1 1f\n\ts_waitcnt vmcnt(8)\n1:" :: "s"(fq), "s"(c.tag) : "memory", "scc"); }
      else asm volatile("s_cmp_eq_u32 %0, %1\n\ts_cbranch_scc1 1f\n\ts_waitcnt vmcnt(0)\n1:" :: "s"(fq), "s"(c.tag) : "memory", "scc");
      asm volatile("" : "+v"(pu[0]), "+v"(pu[1]), "+v"(pu[2]));
      asm volatile("s_load_dwordx16 %0, %1, %2 glc" : "=&s"(fv) : "s"(p.ws), "s"(kf) : "memory");
      unsigned wv[20];
      f32x2 u[10], e[10];
      const unsigned kz = pu[2].z;
      const float k = __builtin_bit_cast(float, kz);
#pragma unroll
      for (int i = 0; i < 10; ++i) { const unsigned x = pu[i >> 2][i & 3]; u[i] = f32x2{(float)(x & 0xffff), (float)(x >> 16)} * f32x2{k, k}; e[i] = u[i] - f32x2{65535.f * k, 65535.f * k}; }
      soft20(e, wv);
      asm volatile("" : "+v"(wv[0]), "+v"(wv[1]), "+v"(wv[2]), "+v"(wv[3]), "+v"(wv[4]), "+v"(wv[5]), "+v"(wv[6]), "+v"(wv[7]), "+v"(wv[8]), "+v"(wv[9]));
      asm volatile("s_waitcnt lgkmcnt(0)" : "+s"(fv)::"memory");
      if (late) {
        if (differs<16>(fv, c.tag)) wait_flags<16>(p.ws, c.rw, kf, 0, c.tag);
        K_ISSUE();
      }
      SB();
#pragma unroll
      for (int i = 0; i < 10; ++i) e[i] = -u[i];
      soft20(e, wv + 10);
      v4u qr[4];
#pragma unroll
      for (int i = 0; i < 4; ++i) {
        swap32(wv[i], wv[8 + i]); swap32(wv[4 + i], wv[12 + i]); swap16(wv[i], wv[4 + i]); swap16(wv[8 + i], wv[12 + i]);
        qr[i] = g == i ? v4u{wv[16], wv[17], wv[18], wv[19]} : v4u{0, 0, 0, 0};
      }
      asm volatile("" : "+v"(qr[0]), "+v"(qr[1]), "+v"(qr[2]), "+v"(qr[3]));
      SB();
      K_GET();
      f32x4 sa = mfma32(sl, sub4(ka, 0), f32x4{0, 0, 0, 0}), sb = mfma32(sl, sub4(kb, 0), f32x4{0, 0, 0, 0});
#pragma unroll
      for (int i = 1; i < 4; ++i) { sa = mfma32(sl, sub4(ka, i), sa); sb = mfma32(sl, sub4(kb, i), sb); }
      *(LAS v2u*)(L + kw) = pk4(add4<SC2>(sa, sb));
      arrive(C_K);
      await(L, C_K, 4);
      const v4u A00 = *(LAS v4u*)(L + r0), A01 = *(LAS v4u*)(L + r1);
      const v2u x0 = *(LAS v2u*)(L + r2), x1 = *(LAS v2u*)(L + r2 + 192), y0 = *(LAS v2u*)(L + r3), y1 = *(LAS v2u*)(L + r3 + 192);
      const v4u A10 = {x0.x, x0.y, x1.x, x1.y}, A11 = {y0.x, y0.y, y1.x, y1.y};
      f32x4 o0[4], o1[4];
#pragma unroll
      for (int i = 0; i < 4; ++i) { const v4u qm = {wv[4 * i], wv[4 * i + 1], wv[4 * i + 2], wv[4 * i + 3]}; o0[i] = mfma32(A00, qm, f32x4{0, 0, 0, 0}); o1[i] = mfma32(A01, qm, f32x4{0, 0, 0, 0}); }
#pragma unroll
      for (int i = 0; i < 4; ++i) { o0[i] = mfma32(A10, qr[i], o0[i]); o1[i] = mfma32(A11, qr[i], o1[i]); }
      if (OE) { SB(); asm volatile("s_load_dwordx8 %0, %1, %2 glc" : "=&s"(fo8) : "s"(p.ws), "s"(MB_O + (rt * 8 + ct) * 256 + aw * 32) : "memory"); }
      v2u rm[2];
#pragma unroll
      for (int i = 0; i < 2; ++i) {
        v4u a0 = __builtin_bit_cast(v4u, o0[2 * i]), b0 = __builtin_bit_cast(v4u, o0[2 * i + 1]), a1 = __builtin_bit_cast(v4u, o1[2 * i]), b1 = __builtin_bit_cast(v4u, o1[2 * i + 1]);
#pragma unroll
        for (int j = 0; j < 4; ++j) { unsigned x = a1[j], y = b1[j]; swap32(x, y); a1[j] = x; b1[j] = y; x = a0[j]; y = b0[j]; swap32(x, y); a0[j] = x; b0[j] = y; }
        const float rd = __builtin_amdgcn_rcpf(__builtin_bit_cast(float, b1.x) + 1e-6f);
        st16(c.rw, oa + i * 2 * NKS * 1024, pk8(mul4<SC2>(__builtin_bit_cast(f32x4, a0), rd), mul4<SC2>(__builtin_bit_cast(f32x4, a1), rd)));
        rm[i] = pk4(mul4<SC2>(__builtin_bit_cast(f32x4, b0), rd));
      }
      unsigned m0 = rm[0].x, m1 = rm[0].y, n0 = rm[1].x, n1 = rm[1].y;
      swap16(m0, n0); swap16(m1, n1);
      st8(c.rw, oc, v2u{m0, m1});
      SB();
      st4(c.rw, of, c.tag);
    }
#pragma unroll
    for (int j = 0; j < 5; ++j) wpre[4][j] = ld16s(c.rw, lane_id() * 16, WS_WT + ((ct * 5 + j) * NKS + 70 + aw) * 1024);
    SB();
    unsigned ol = 1;
    if (OE) { asm volatile("s_waitcnt lgkmcnt(0)" : "+s"(fo8)::"memory"); ol = differs<8>(fo8, c.tag); }
    {
      v4u of[4][5]; unsigned t0, it;
      const int lane = lane_id();
      const unsigned afr = WS_AFR + rt * 4 * NKS * 1024 + lane * 16;
      const unsigned fo = afr + (54 + 4 * aw) * 1024, ro = afr + (70 + aw) * 1024;
      unsigned so[4];
#pragma unroll
      for (int n = 0; n < 4; ++n) so[n] = ((aw + RTB(ct) + 1 + n) & 3) * NKS * 1024;
      const int xw = (aw + RTB(ct) - RTA(ct)) & 3;
      __builtin_amdgcn_s_setprio(PB1);
      const unsigned mb0 = (rt * 8 + ct) * 256;
      const unsigned xf = sel(lane == 0, MB_X + (mb0 ^ 2048) + aw * 4, OOB);
      {
        v8u fv;
        await(L, C_S + 4 * aw, 1);
        LAS char* qs = L + L_XB + aw * 15360 + 10240 + lane * 16;
        LAS char* qr = L + L_RES + aw * 3072 + (lane >> 4) * 8;
        const unsigned xa = WS_XCH + (mb0 ^ 2048) * 80 + aw * 5120 + lane * 16;
#pragma unroll
        for (int j = 0; j < 5; ++j) {
          const v2u xv = *(LAS v2u*)(qr + j * 32 + (lane & 15) * 160), bv = *(LAS v2u*)(qr + j * 32 + 2560);
          const f32x4 v = add4<SC3>(*(LAS f32x4*)(qs + j * 1024), add4<SC3>(f32x4{lo(xv.x), hi(xv.x), lo(xv.y), hi(xv.y)}, f32x4{lo(bv.x), hi(bv.x), lo(bv.y), hi(bv.y)}));
          st16(c.rw, xa + j * 1024, __builtin_bit_cast(v4u, v));
        }
        SB();
        __builtin_amdgcn_s_waitcnt(0x0f75);
        if (ol) {
          asm volatile("s_load_dwordx8 %0, %1, %2 glc\n\ts_waitcnt lgkmcnt(0)" : "=&s"(fv) : "s"(p.ws), "s"(MB_O + mb0 + aw * 32) : "memory");
          if (differs<8>(fv, c.tag)) {
            asm volatile("s_load_dwordx8 %0, %1, %2 glc\n\ts_waitcnt vmcnt(0)\n\tbuffer_store_dword %3, %4, %5, 0 offen sc1\n\ts_waitcnt lgkmcnt(0)" : "=&s"(fv) : "s"(p.ws), "s"(MB_O + mb0 + aw * 32), "v"(c.tag), "v"(xf), "s"(c.rw) : "memory");
            if (differs<8>(fv, c.tag)) wait_flags<8>(p.ws, c.rw, MB_O + mb0 + aw * 32, 0, c.tag);
          }
        }
      }
      if (RPOS) await(L, C_R, 4);
      SB();
      O_ISSUE0(); O_ISSUE1(); O_ISSUE2();
      if (XO) asm volatile("s_waitcnt vmcnt(15)\n\tbuffer_store_dword %0, %1, %2, 0 offen sc1" :: "v"(c.tag), "v"(xf), "s"(c.rw) : "memory");
      f32x4 acc[2][5], t[5], u[5], x[5], y[5];
      unsigned cn, cx, fx;
      LAS char* q = L + L_XB + aw * 15360 + lane * 16;
      LAS char* qx = L + ((xw + 3) & 3) * AW + lane * 16;
      auto chk = [&](int n, int k) { t0 = max3u(k ? t0 : 0u, max3u(of[n][k].x, of[n][k].y, of[n][k].z), of[n][k].w); };
      unsigned po4, po2; int s4, s2;
      O_WAIT0();
#pragma unroll
      for (int k = 0; k < 5; ++k) chk(0, k);
      O_FIX0();
      sfor<0, 4>([&](auto N) {
        constexpr int n = N.v, a = n & 1;
        LAS char* d = L + L_XB + (((aw + n) & 3) * 3 + n - 1) * 5120 + lane * 16;
#pragma unroll
        for (int m = 0; m < 25; ++m) {
          const int k = m / 5, j = m % 5;
          acc[a][j] = mfma32(wpre[k][j], of[n][k], k || n == 3 ? acc[a][j] : f32x4{0, 0, 0, 0});
          if (n == 0) { if (m == 0) O_LD30(); if (m == 4) O_LD31(); if (m == 8) O_LD32(); if (m == 12) O_LD33(); if (m == 16) O_LD34(); }
          if (n == 1 || n == 2) { if (m >= 2 && m <= 10 && m % 2 == 0) *(LAS f32x4*)(d + (m / 2 - 1) * 1024) = acc[a ^ 1][m / 2 - 1]; if (m == 11) arrive(C_B + 4 * (n - 1)); }
          if (n < 3 && m == 18) { if (n == 0) O_WAIT1(); else if (n == 1) O_WAIT2(); else O_WAIT3(); }
          if (n < 3 && m >= 19 && m < 24) chk(n + 1, m - 19);
          if (n == 1 && m == 12) asm volatile("s_load_dword %0, %1, %2 glc" : "=s"(fx) : "s"(p.ws), "s"(MB_X + mb0 + xw * 4) : "memory");
          if (n == 2 && m == XM) asm volatile("s_mov_b32 %1, 0x100000\n1:\ts_waitcnt lgkmcnt(0)\n\ts_cmp_eq_u32 %0, %4\n\ts_cbranch_scc1 2f\n\ts_load_dword %0, %2, %3 glc\n\ts_sub_u32 %1, %1, 1\n\ts_cbranch_scc0 1b\n\ts_waitcnt lgkmcnt(0)\n2:"
                                              : "+s"(fx), "=&s"(it) : "s"(p.ws), "s"(MB_X + mb0 + xw * 4), "s"(c.tag) : "memory", "scc");
          if (n == 2 && m >= XM && m < XM + 5) y[m - XM] = __builtin_bit_cast(f32x4, ld16s(c.rw, lane * 16 + (m - XM) * 1024, WS_XCH + mb0 * 80 + xw * 5120));
          if (n == 2) {
            if (m == 9) cn = peekv(L, C_B);
            if (m == 12) recheck(cn, C_B, 4);
            if (m >= 12 && m < 17) acc[1][m - 12] = *(LAS f32x4*)(q + (m - 12) * 1024);
            if (m == 22) cn = peekv(L, C_B + 4);
          }
          if (n == 3) {
            if (m >= 1 && m < 6) *(LAS f32x4*)(d + (m - 1) * 1024) = acc[0][m - 1];
            if (m == 5) { arrive(C_B + 8); cx = peekv(L, C_X + 4 * xw); recheck(cn, C_B + 4, 4); }
            if (m >= 6 && m < 11) t[m - 6] = *(LAS f32x4*)(q + 5120 + (m - 6) * 1024);
            if (m == 11) recheckv(cx, C_X + 4 * xw, 1);
            if (m >= 11 && m < 16) x[m - 11] = *(LAS f32x4*)(qx + (m - 11) * 1024);
            if (m == 13) cn = peekv(L, C_B + 8);
            if (m == HM) {
              const int ln = lane_id();
              const unsigned po = (rt * 64 + ((aw + RTB(ct)) & 3) * 16 + (ln >> 2)) * (D * 2) + ct * 160;
              po4 = po + (ln & 3) * 16; po2 = po + (ln & 3) * 8; s4 = (ln & 60) + (ln & 1) * 128 + (ln & 2) * 32; s2 = (ln & 60) + (ln & 3) * 64;
              asm volatile("" : "+v"(po4), "+v"(po2), "+v"(s4), "+v"(s2));
            }
            if (m == 17) recheck(cn, C_B + 8, 4);
            if (m >= 17 && m < 22) u[m - 17] = *(LAS f32x4*)(q + 10240 + (m - 17) * 1024);
            if (m >= 20) t[m - 20] = add4<SC4>(t[m - 20], x[m - 20]);
          }
          SB();
        }
        if (n == 0) O_FIX1(); else if (n == 1) O_FIX2(); else if (n == 2) O_FIX3();
      });
#pragma unroll
      for (int j = 0; j < 5; ++j) t[j] += u[j] + y[j];
      asm volatile("hk_end1:");
      const rsrc_t rso = __builtin_amdgcn_make_buffer_rsrc(p.out, 0, S * D * 2, 0x00020000);
      v2u pv[5];
#pragma unroll
      for (int j = 0; j < 5; ++j) pv[j] = pk4(acc[1][j] + t[j]);
#define OUT_ST(o)                                                                                                                         \
  _Pragma("unroll") for (int j = o; j < 4 + o; j += 2) {                                                                                  \
    unsigned x0 = pv[j].x, x1 = pv[j].y, y0 = pv[j + 1].x, y1 = pv[j + 1].y;                                                              \
    swap16(x0, y0); swap16(x1, y1);                                                                                                       \
    if (OP) { x0 = BP(s4, x0); x1 = BP(s4, x1); y0 = BP(s4, y0); y1 = BP(s4, y1); }                                                      \
    __builtin_amdgcn_raw_buffer_store_b128(v4u{x0, x1, y0, y1}, rso, po4 + j * 32, 0, OA);   \
  }                                                                                                                                       \
  __builtin_amdgcn_raw_buffer_store_b64(OP ? v2u{BP(s2, pv[o ? 0 : 4].x), BP(s2, pv[o ? 0 : 4].y)} : pv[o ? 0 : 4], rso, po2 + (o ? 0 : 128), 0, OB)
      if (OAL && (ct & 1)) { OUT_ST(1); } else { OUT_ST(0); }
    }
  }
}
__global__ __launch_bounds__(512, 2) void fk(P p) {
  const int w = __builtin_amdgcn_readfirstlane(threadIdx.x >> 6);
  unsigned cw[4];
  {
    u64 pc; unsigned n;
    asm volatile("s_getpc_b64 %0\nhk_beg:\ts_mov_b32 %1, hk_end0-hk_beg\n\ts_max_i32 %1, %1, hk_end1-hk_beg" : "=s"(pc), "=s"(n)::"scc");
    pc += __builtin_elementwise_min(((blockIdx.x >> 3) * 8 + w) * 256, n - 192);
    asm volatile("s_load_dword %0, %4, 0\n\ts_load_dword %1, %4, 64\n\ts_load_dword %2, %4, 128\n\ts_load_dword %3, %4, 192" : "=&s"(cw[0]), "=&s"(cw[1]), "=&s"(cw[2]), "=&s"(cw[3]) : "s"(pc) : "memory");
  }
  if (w < 4) { run<false>(p, w, cw); __builtin_amdgcn_endpgm(); }
  run<true>(p, w, cw);
}
}

extern "C" void kernel_launch(void* const* d_in, const int* in_sizes, int n_in, void* d_out, int out_size, void* d_ws, size_t ws_size, hipStream_t stream) {
  using namespace hk;
  P p;
  p.x = (const u16*)d_in[0]; p.wqkv = (const u16*)d_in[1]; p.wphi = (const u16*)d_in[2]; p.wo = (const u16*)d_in[3];
  p.w1 = (const u16*)d_in[5]; p.b1 = (const u16*)d_in[6]; p.w2 = (const u16*)d_in[7]; p.b2 = (const u16*)d_in[8];
  p.out = (u16*)d_out; p.ws = (char*)d_ws;
  (void)hipFuncSetAttribute((const void*)fk, hipFuncAttributeMaxDynamicSharedMemorySize, L_BYTES);
  fk<<<256, 512, L_BYTES, stream>>>(p);
}
```
